```python
import torch

def _softmax(x, axis=-1):
    return torch.softmax(x, dim=axis)
def _gelu(x):
    return torch.nn.functional.gelu(x, approximate="tanh")
def _silu(x):
    return torch.nn.functional.silu(x)
def _sigmoid(x):
    return torch.sigmoid(x)
def _layernorm(x, axis=-1, eps=1e-5):
    m = x.mean(dim=axis, keepdim=True)
    v = x.var(dim=axis, keepdim=True, unbiased=False)
    return (x - m) / torch.sqrt(v + eps)
def _scatter_add(tgt, idx, src, axis=0):
    return tgt.clone().index_add_(axis, idx, src)
def _segment_sum(src, seg, n):
    out = torch.zeros((n,) + src.shape[1:], dtype=src.dtype)
    return out.index_add_(0, seg, src)
def _pad(x, pw):
    flat = tuple(v for pair in reversed(pw) for v in pair)
    return torch.nn.functional.pad(x, flat)

kernel_name = 'interleave_layers__linear_kernel_hedgehog__glu_gated_geglu__prompt'

def setup_inputs():

    _w = (lambda a: (lambda t: t.to(torch.bfloat16) if t.is_floating_point() else t)(torch.as_tensor(a)))
    def _A_setup():
        B, S, D, H = 1, 2048, 640, 32
        Dh = D // H
        return {
            "x": _w((lambda *s: torch.randn(*s, dtype=torch.bfloat16) * 0.05)(B, S, D)),
            "wqkv": _w((lambda *s: torch.randn(*s, dtype=torch.bfloat16) * 0.4)(D, 3 * D)),
            "w_phi": _w((lambda *s: torch.randn(*s, dtype=torch.bfloat16) * 0.4)(H, Dh, Dh) * (Dh ** -0.5)),
            "wo": _w((lambda *s: torch.randn(*s, dtype=torch.bfloat16) * 0.05)(D, D)),
        }
    _A_in = _A_setup()

    def _B_setup():
        B, S, Dm, Df = 1, 2048, 640, 1728
        return {
            "x": _w((lambda *s: torch.randn(*s, dtype=torch.bfloat16) * 0.05)(B, S, Dm)),
            "w_gate": _w((lambda *s: torch.randn(*s, dtype=torch.bfloat16) * 1.6)(Dm, Df) * (Dm ** -0.5)),
            "w_up": _w((lambda *s: torch.randn(*s, dtype=torch.bfloat16) * 1.6)(Dm, Df) * (Dm ** -0.5)),
            "w_down": _w((lambda *s: torch.randn(*s, dtype=torch.bfloat16) * 1.6)(Df, Dm) * (Df ** -0.5)),
        }
    _B_in = _B_setup()

    return {
        "A_x": _A_in["x"],
        "A_wqkv": _A_in["wqkv"],
        "A_w_phi": _A_in["w_phi"],
        "A_wo": _A_in["wo"],
        "B_x": _B_in["x"],
        "B_w_gate": _B_in["w_gate"],
        "B_w_up": _B_in["w_up"],
        "B_w_down": _B_in["w_down"],
    }

def reference(A_x, A_wqkv, A_w_phi, A_wo, B_x, B_w_gate, B_w_up, B_w_down):
    def _A_ref(x, wqkv, w_phi, wo):
        B, S, D = x.shape
        H = 32; Dh = D // H
        qkv = (lambda x, *d: x.permute(*d))((x @ wqkv).reshape(B, S, 3, H, Dh), 2, 0, 3, 1, 4)
        q, k, v = qkv[0], qkv[1], qkv[2]
        pq = torch.einsum("bhsd,hdf->bhsf", q, w_phi)
        pk = torch.einsum("bhsd,hdf->bhsf", k, w_phi)
        phi_q = torch.concatenate([_softmax(pq, axis=-1), _softmax(-pq, axis=-1)], axis=-1)
        phi_k = torch.concatenate([_softmax(pk, axis=-1), _softmax(-pk, axis=-1)], axis=-1)
        kv = torch.einsum("bhsf,bhsd->bhfd", phi_k, v)
        z  = torch.einsum("bhsf->bhf", phi_k)
        num = torch.einsum("bhqf,bhfd->bhqd", phi_q, kv)
        den = torch.einsum("bhqf,bhf->bhq", phi_q, z)[..., None] + 1e-6
        o = num / den
        return (lambda x, *d: x.permute(*d))(o, 0, 2, 1, 3).reshape(B, S, D) @ wo

    def _B_ref(x, w_gate, w_up, w_down):
        g = torch.einsum("bsd,df->bsf", x, w_gate)
        u = torch.einsum("bsd,df->bsf", x, w_up)
        h = _gelu(g) * u
        return torch.einsum("bsf,fd->bsd", h, w_down)

    x = A_x
    x = x + _A_ref(_layernorm(x, axis=-1), A_wqkv, A_w_phi, A_wo)
    x = x + _B_ref(_layernorm(x, axis=-1), B_w_gate, B_w_up, B_w_down)
    x = x + _A_ref(_layernorm(x, axis=-1), A_wqkv, A_w_phi, A_wo)
    x = x + _B_ref(_layernorm(x, axis=-1), B_w_gate, B_w_up, B_w_down)
    return x
```

```cpp
#include <hip/hip_runtime.h>
#include <hip/hip_bf16.h>
#include <stdint.h>

typedef __attribute__((ext_vector_type(8))) short bf16x8;
typedef __attribute__((ext_vector_type(4))) float f32x4;
typedef __attribute__((ext_vector_type(4))) unsigned u32x4;
typedef __attribute__((ext_vector_type(2))) unsigned u32x2;
typedef __attribute__((ext_vector_type(2))) __bf16 bf16x2_t;

namespace {

constexpr int S_ = 2048, D_ = 640, H_ = 32, DF_ = 1728;

constexpr size_t WQKV_T = 0;
constexpr size_t WQKV_B = (size_t)8 * 3 * 20 * 5 * 1024;
constexpr size_t WO_T = WQKV_T + WQKV_B;
constexpr size_t WO_B = (size_t)8 * 20 * 5 * 1024;
constexpr size_t WGU_T = WO_T + WO_B;
constexpr size_t WGU_B = (size_t)108 * 20 * 2 * 1024;
constexpr size_t WD_T = WGU_T + WGU_B;
constexpr size_t WD_B = (size_t)8 * 54 * 5 * 1024;
constexpr size_t PHIQ = WD_T + WD_B;
constexpr size_t PHIQ_B = (size_t)32 * 128 * 1280;
constexpr size_t KVP = PHIQ + PHIQ_B;
constexpr size_t KVP_B = (size_t)32 * 32 * 960 * 4;
constexpr size_t O_T = KVP + KVP_B;
constexpr size_t O_B = (size_t)S_ * D_ * 2;
constexpr size_t H_T = O_T + O_B;
constexpr size_t H_B = (size_t)S_ * DF_ * 2;
constexpr size_t XA = H_T + H_B;
constexpr size_t XB = XA + O_B;
constexpr size_t WS_END = XB + O_B;

constexpr int N_PIECE_QKV = 8 * 3 * 20 * 5 * 64;
constexpr int N_PIECE_WO = 8 * 20 * 5 * 64;
constexpr int N_PIECE_GU = 108 * 20 * 2 * 64;
constexpr int N_PIECE_WD = 8 * 54 * 5 * 64;
constexpr int N_PIECE = N_PIECE_QKV + N_PIECE_WO + N_PIECE_GU + N_PIECE_WD;

__device__ __forceinline__ unsigned pack2(float a, float b) {
  bf16x2_t v;
  v[0] = (__bf16)a;
  v[1] = (__bf16)b;
  return __builtin_bit_cast(unsigned, v);
}
__device__ __forceinline__ float lo2f(unsigned u) { return __uint_as_float(u << 16); }
__device__ __forceinline__ float hi2f(unsigned u) { return __uint_as_float(u & 0xffff0000u); }
__device__ __forceinline__ float rbf(float a) {
  bf16x2_t v;
  v[0] = (__bf16)a;
  v[1] = (__bf16)0.f;
  return lo2f(__builtin_bit_cast(unsigned, v));
}
__device__ __forceinline__ float us2f(unsigned short u) { return __uint_as_float((unsigned)u << 16); }

__device__ __forceinline__ f32x4 mfma16(u32x4 a, u32x4 b, f32x4 c) {
  return __builtin_amdgcn_mfma_f32_16x16x32_bf16(__builtin_bit_cast(bf16x8, a), __builtin_bit_cast(bf16x8, b), c, 0, 0,
                                                 0);
}

__global__ __launch_bounds__(256) void k_prep(const unsigned short* __restrict__ wqkv,
                                              const unsigned short* __restrict__ wphi,
                                              const unsigned short* __restrict__ wo,
                                              const unsigned short* __restrict__ wg,
                                              const unsigned short* __restrict__ wu,
                                              const unsigned short* __restrict__ wd, unsigned char* __restrict__ ws) {
  int p = blockIdx.x * 256 + threadIdx.x;
  if (p >= N_PIECE) return;
  float v[8];
  u32x4* dst;
  if (p < N_PIECE_QKV) {
    int lane = p & 63, fr = p >> 6;
    int cg = fr % 5, ks = (fr / 5) % 20, t = (fr / 100) % 3, g = fr / 300;
    int i = lane & 15, q = lane >> 4;
    int head = 4 * g + (i >> 2), f = cg * 4 + (i & 3);
    int k0 = ks * 32 + 8 * q;
    if (t < 2) {
      float ph[20];
#pragma unroll
      for (int d = 0; d < 20; ++d) ph[d] = us2f(wphi[head * 400 + d * 20 + f]);
#pragma unroll
      for (int j = 0; j < 8; ++j) {
        const unsigned short* wr = wqkv + (size_t)(k0 + j) * 1920 + t * 640 + head * 20;
        float s = 0.f;
#pragma unroll
        for (int d = 0; d < 20; ++d) s = fmaf(us2f(wr[d]), ph[d], s);
        v[j] = s;
      }
    } else {
#pragma unroll
      for (int j = 0; j < 8; ++j) v[j] = us2f(wqkv[(size_t)(k0 + j) * 1920 + 1280 + head * 20 + f]);
    }
    dst = (u32x4*)(ws + WQKV_T) + p;
  } else if (p < N_PIECE_QKV + N_PIECE_WO) {
    int pp = p - N_PIECE_QKV;
    int lane = pp & 63, fr = pp >> 6;
    int cg = fr % 5, ks = (fr / 5) % 20, n = fr / 100;
    int i = lane & 15, q = lane >> 4;
    int col = n * 80 + cg * 16 + i;
#pragma unroll
    for (int j = 0; j < 8; ++j) {
      int half = j >> 2, r = j & 3, head, d;
      if (ks < 16) {
        head = 2 * ks + half;
        d = 4 * q + r;
      } else {
        head = 8 * (ks - 16) + 2 * q + half;
        d = 16 + r;
      }
      v[j] = us2f(wo[(size_t)(head * 20 + d) * 640 + col]);
    }
    dst = (u32x4*)(ws + WO_T) + pp;
  } else if (p < N_PIECE_QKV + N_PIECE_WO + N_PIECE_GU) {
    int pp = p - N_PIECE_QKV - N_PIECE_WO;
    int lane = pp & 63, fr = pp >> 6;
    int gu = fr & 1, ks = (fr >> 1) % 20, c = fr / 40;
    int i = lane & 15, q = lane >> 4;
    const unsigned short* w = gu ? wu : wg;
    int k0 = ks * 32 + 8 * q;
#pragma unroll
    for (int j = 0; j < 8; ++j) v[j] = us2f(w[(size_t)(k0 + j) * DF_ + c * 16 + i]);
    dst = (u32x4*)(ws + WGU_T) + pp;
  } else {
    int pp = p - N_PIECE_QKV - N_PIECE_WO - N_PIECE_GU;
    int lane = pp & 63, fr = pp >> 6;
    int cg = fr % 5, ks = (fr / 5) % 54, n = fr / 270;
    int i = lane & 15, q = lane >> 4;
    int col = n * 80 + cg * 16 + i;
#pragma unroll
    for (int j = 0; j < 8; ++j) {
      int half = j >> 2, r = j & 3;
      int feat = (2 * ks + half) * 16 + 4 * q + r;
      v[j] = us2f(wd[(size_t)feat * 640 + col]);
    }
    dst = (u32x4*)(ws + WD_T) + pp;
  }
  u32x4 o;
  o[0] = pack2(v[0], v[1]);
  o[1] = pack2(v[2], v[3]);
  o[2] = pack2(v[4], v[5]);
  o[3] = pack2(v[6], v[7]);
  *dst = o;
}

__device__ __forceinline__ void ln_load(const unsigned short* __restrict__ x, int grow0, int lane, u32x4 (&v)[4][5]) {
  int sub = lane >> 4, c16 = lane & 15;
#pragma unroll
  for (int p = 0; p < 4; ++p) {
    const u32x4* src = (const u32x4*)(x + (size_t)(grow0 + p * 4 + sub) * D_) + c16;
#pragma unroll
    for (int i = 0; i < 5; ++i) v[p][i] = src[16 * i];
  }
}
__device__ __forceinline__ float red16(float s) {
  s += __shfl_xor(s, 1);
  s += __shfl_xor(s, 2);
  s += __shfl_xor(s, 4);
  s += __shfl_xor(s, 8);
  return s;
}
__device__ __forceinline__ void ln_store(unsigned char* lds, int trow0, int lane, u32x4 (&v)[4][5]) {
  int sub = lane >> 4, c16 = lane & 15;
#pragma unroll
  for (int p = 0; p < 4; ++p) {
    float s = 0.f;
#pragma unroll
    for (int i = 0; i < 5; ++i)
#pragma unroll
      for (int e = 0; e < 4; ++e) s += lo2f(v[p][i][e]) + hi2f(v[p][i][e]);
    float mean = red16(s) * (1.f / 640.f);
    float q = 0.f;
#pragma unroll
    for (int i = 0; i < 5; ++i)
#pragma unroll
      for (int e = 0; e < 4; ++e) {
        float a = lo2f(v[p][i][e]) - mean, b = hi2f(v[p][i][e]) - mean;
        q += a * a + b * b;
      }
    float rstd = rsqrtf(red16(q) * (1.f / 640.f) + 1e-5f);
    int row = trow0 + p * 4 + sub;
    unsigned char* dst = lds + row * 1280 + ((c16 ^ (row & 15)) << 4);
#pragma unroll
    for (int i = 0; i < 5; ++i) {
      u32x4 o;
#pragma unroll
      for (int e = 0; e < 4; ++e)
        o[e] = pack2((lo2f(v[p][i][e]) - mean) * rstd, (hi2f(v[p][i][e]) - mean) * rstd);
      *(u32x4*)(dst + 256 * i) = o;
    }
  }
}

__device__ __forceinline__ void dual_softmax(const f32x4 (&acc)[5], unsigned (&out)[20]) {
  float p[20];
#pragma unroll
  for (int c = 0; c < 5; ++c)
#pragma unroll
    for (int r = 0; r < 4; ++r) p[4 * c + r] = rbf(acc[c][r]);
  float mx = p[0], mn = p[0];
#pragma unroll
  for (int f = 1; f < 20; ++f) {
    mx = fmaxf(mx, p[f]);
    mn = fminf(mn, p[f]);
  }
  float e[20], n[20], s1 = 0.f, s2 = 0.f;
#pragma unroll
  for (int f = 0; f < 20; ++f) {
    e[f] = __expf(p[f] - mx);
    n[f] = __expf(mn - p[f]);
    s1 += e[f];
    s2 += n[f];
  }
  float r1 = 1.f / s1, r2 = 1.f / s2;
#pragma unroll
  for (int f = 0; f < 10; ++f) {
    out[f] = pack2(e[2 * f] * r1, e[2 * f + 1] * r1);
    out[10 + f] = pack2(n[2 * f] * r2, n[2 * f + 1] * r2);
  }
}

constexpr int K1_PHI_OFF = 64 * 1280;
constexpr int K1_V_OFF = K1_PHI_OFF + 4 * 48 * 144;
constexpr int K1_LDS = K1_V_OFF + 4 * 32 * 144;
constexpr int K1_PF = 6;

__global__ __launch_bounds__(256, 1) void k_qkv(const unsigned short* __restrict__ x, unsigned char* __restrict__ ws) {
  extern __shared__ __attribute__((aligned(16))) unsigned char lds[];
  const int b = blockIdx.x, lane = threadIdx.x & 63;
  const int wave = __builtin_amdgcn_readfirstlane(threadIdx.x >> 6);
  const int mt = (b & 7) * 4 + ((b >> 3) >> 3), g = (b >> 3) & 7;
  const int tok = lane & 15, q = lane >> 4;

  u32x4 xv[4][5];
  ln_load(x, mt * 64 + wave * 16, lane, xv);

  const int t = wave < 3 ? wave : 0;
  const u32x4* wp = (const u32x4*)(ws + WQKV_T) + (size_t)((g * 3 + t) * 100) * 64 + lane;
  u32x4 wr[K1_PF][5];
  if (wave < 3) {
#pragma unroll
    for (int s = 0; s < K1_PF; ++s)
#pragma unroll
      for (int c = 0; c < 5; ++c) wr[s][c] = wp[(s * 5 + c) * 64];
  }
  __builtin_amdgcn_sched_barrier(0);
  ln_store(lds, wave * 16, lane, xv);
  __syncthreads();

  if (wave < 3) {
    f32x4 acc[4][5];
#pragma unroll
    for (int rg = 0; rg < 4; ++rg)
#pragma unroll
      for (int c = 0; c < 5; ++c) acc[rg][c] = f32x4{0.f, 0.f, 0.f, 0.f};
    const unsigned char* abase = lds + tok * 1280;
    int aoff[4];
#pragma unroll
    for (int m = 0; m < 4; ++m) aoff[m] = ((m << 2) ^ (q ^ tok)) << 4;
#pragma unroll
    for (int ks = 0; ks < 20; ++ks) {
      u32x4 a[4];
#pragma unroll
      for (int rg = 0; rg < 4; ++rg)
        a[rg] = *(const u32x4*)(abase + aoff[ks & 3] + (ks >> 2) * 256 + rg * 16 * 1280);
#pragma unroll
      for (int c = 0; c < 5; ++c)
#pragma unroll
        for (int rg = 0; rg < 4; ++rg) acc[rg][c] = mfma16(wr[ks % K1_PF][c], a[rg], acc[rg][c]);
      if (ks + K1_PF < 20) {
#pragma unroll
        for (int c = 0; c < 5; ++c) wr[ks % K1_PF][c] = wp[((ks + K1_PF) * 5 + c) * 64];
      }
      __builtin_amdgcn_sched_barrier(0);
    }
    if (wave == 0) {
      const int head = g * 4 + q;
#pragma unroll
      for (int rg = 0; rg < 4; ++rg) {
        unsigned o[20];
        dual_softmax(acc[rg], o);
        u32x4* dst = (u32x4*)(ws + PHIQ + ((size_t)head * 128 + mt * 4 + rg) * 1280) + tok;
#pragma unroll
        for (int c = 0; c < 5; ++c) dst[16 * c] = u32x4{o[4 * c], o[4 * c + 1], o[4 * c + 2], o[4 * c + 3]};
      }
    } else if (wave == 1) {
#pragma unroll
      for (int rg = 0; rg < 4; ++rg) {
        unsigned o[20];
        dual_softmax(acc[rg], o);
        unsigned short* dst = (unsigned short*)(lds + K1_PHI_OFF + q * (48 * 144) + (rg * 16 + tok) * 2);
#pragma unroll
        for (int f = 0; f < 20; ++f) {
          dst[(2 * f) * 72] = (unsigned short)(o[f] & 0xffffu);
          dst[(2 * f + 1) * 72] = (unsigned short)(o[f] >> 16);
        }
      }
    } else {
#pragma unroll
      for (int rg = 0; rg < 4; ++rg) {
        unsigned short* dst = (unsigned short*)(lds + K1_V_OFF + q * (32 * 144) + (rg * 16 + tok) * 2);
#pragma unroll
        for (int c = 0; c < 5; ++c) {
          unsigned p0 = pack2(acc[rg][c][0], acc[rg][c][1]), p1 = pack2(acc[rg][c][2], acc[rg][c][3]);
          dst[(4 * c + 0) * 72] = (unsigned short)(p0 & 0xffffu);
          dst[(4 * c + 1) * 72] = (unsigned short)(p0 >> 16);
          dst[(4 * c + 2) * 72] = (unsigned short)(p1 & 0xffffu);
          dst[(4 * c + 3) * 72] = (unsigned short)(p1 >> 16);
        }
        dst[20 * 72] = 0x3f80;
        dst[21 * 72] = 0;
        dst[22 * 72] = 0;
        dst[23 * 72] = 0;
      }
    }
  }
  __syncthreads();
  {
    const unsigned char* vb = lds + K1_V_OFF + wave * (32 * 144) + tok * 144 + q * 16;
    const unsigned char* pb = lds + K1_PHI_OFF + wave * (48 * 144) + tok * 144 + q * 16;
    f32x4 d[2][3];
#pragma unroll
    for (int dt = 0; dt < 2; ++dt)
#pragma unroll
      for (int ft = 0; ft < 3; ++ft) d[dt][ft] = f32x4{0.f, 0.f, 0.f, 0.f};
#pragma unroll
    for (int ks = 0; ks < 2; ++ks) {
      u32x4 va[2], pf[3];
#pragma unroll
      for (int dt = 0; dt < 2; ++dt) va[dt] = *(const u32x4*)(vb + dt * 16 * 144 + ks * 64);
#pragma unroll
      for (int ft = 0; ft < 3; ++ft) pf[ft] = *(const u32x4*)(pb + ft * 16 * 144 + ks * 64);
#pragma unroll
      for (int dt = 0; dt < 2; ++dt)
#pragma unroll
        for (int ft = 0; ft < 3; ++ft) d[dt][ft] = mfma16(va[dt], pf[ft], d[dt][ft]);
    }
    float* dst = (float*)(ws + KVP) + ((size_t)mt * 32 + g * 4 + wave) * 960;
#pragma unroll
    for (int ft = 0; ft < 3; ++ft) {
      int f = ft * 16 + tok;
      if (f < 40) {
        *(f32x4*)(dst + f * 24 + 4 * q) = d[0][ft];
        if (q < 2) *(f32x4*)(dst + f * 24 + 16 + 4 * q) = d[1][ft];
      }
    }
  }
}

__global__ __launch_bounds__(256) void k_attn_out(unsigned char* __restrict__ ws) {
  __shared__ __attribute__((aligned(16))) unsigned short skv[40 * 24];
  const int b = blockIdx.x, tid = threadIdx.x, lane = tid & 63;
  const int wave = __builtin_amdgcn_readfirstlane(tid >> 6);
  const int mt256 = b & 7, head = b >> 3;
  const int tok = lane & 15, q = lane >> 4;

  u32x4 pq0[4], pq1[4];
#pragma unroll
  for (int i = 0; i < 4; ++i) {
    int rgG = mt256 * 16 + wave * 4 + i;
    const u32x4* src = (const u32x4*)(ws + PHIQ + ((size_t)head * 128 + rgG) * 1280);
    pq0[i] = src[lane];
    u32x4 z = {0u, 0u, 0u, 0u};
    pq1[i] = z;
    if (q == 0) pq1[i] = src[64 + tok];
  }
  if (tid < 240) {
    const f32x4* src = (const f32x4*)(ws + KVP) + (size_t)head * 240 + tid;
    f32x4 s = {0.f, 0.f, 0.f, 0.f};
#pragma unroll
    for (int p = 0; p < 32; ++p) s += src[(size_t)p * 32 * 240];
    u32x2 o;
    o[0] = pack2(s[0], s[1]);
    o[1] = pack2(s[2], s[3]);
    *(u32x2*)(skv + tid * 4) = o;
  }
  __syncthreads();
  u32x4 ka[2][2];
#pragma unroll
  for (int dt = 0; dt < 2; ++dt)
#pragma unroll
    for (int ks = 0; ks < 2; ++ks) {
      unsigned short e[8];
#pragma unroll
      for (int j = 0; j < 8; ++j) {
        int f = ks * 32 + 8 * q + j, d = dt * 16 + tok;
        bool ok = (f < 40) && (d < 24);
        e[j] = ok ? skv[(ok ? f : 0) * 24 + (ok ? d : 0)] : (unsigned short)0;
      }
      ka[dt][ks] = u32x4{(unsigned)e[0] | ((unsigned)e[1] << 16), (unsigned)e[2] | ((unsigned)e[3] << 16),
                         (unsigned)e[4] | ((unsigned)e[5] << 16), (unsigned)e[6] | ((unsigned)e[7] << 16)};
    }
#pragma unroll
  for (int i = 0; i < 4; ++i) {
    f32x4 d0 = {0.f, 0.f, 0.f, 0.f}, d1 = {0.f, 0.f, 0.f, 0.f};
    d0 = mfma16(ka[0][0], pq0[i], d0);
    d0 = mfma16(ka[0][1], pq1[i], d0);
    d1 = mfma16(ka[1][0], pq0[i], d1);
    d1 = mfma16(ka[1][1], pq1[i], d1);
    float den = __shfl(d1[0], 16 + tok);
    den = rbf(rbf(den) + 1e-6f);
    float rd = 1.f / den;
    int rgG = mt256 * 16 + wave * 4 + i;
    int mt64 = rgG >> 2, rg = rgG & 3;
    {
      u32x2 o;
      o[0] = pack2(rbf(d0[0]) * rd, rbf(d0[1]) * rd);
      o[1] = pack2(rbf(d0[2]) * rd, rbf(d0[3]) * rd);
      unsigned char* dst = ws + O_T + (((size_t)mt64 * 20 + (head >> 1)) * 4 + rg) * 1024 + lane * 16 + (head & 1) * 8;
      *(u32x2*)dst = o;
    }
    if (q == 0) {
      u32x2 o;
      o[0] = pack2(rbf(d1[0]) * rd, rbf(d1[1]) * rd);
      o[1] = pack2(rbf(d1[2]) * rd, rbf(d1[3]) * rd);
      unsigned char* dst = ws + O_T + (((size_t)mt64 * 20 + 16 + (head >> 3)) * 4 + rg) * 1024 +
                           (((head & 7) >> 1) * 16 + tok) * 16 + (head & 1) * 8;
      *(u32x2*)dst = o;
    }
  }
}

template <int NKS, int PF>
__device__ __forceinline__ void splitk_body(const u32x4* __restrict__ wp, const u32x4* __restrict__ ap,
                                            f32x4 (&acc)[5][4]) {
  u32x4 wr[PF][5], ar[PF][4];
#pragma unroll
  for (int s = 0; s < PF; ++s) {
    if (s < NKS) {
#pragma unroll
      for (int rg = 0; rg < 4; ++rg) ar[s][rg] = ap[(s * 4 + rg) * 64];
#pragma unroll
      for (int c = 0; c < 5; ++c) wr[s][c] = wp[(s * 5 + c) * 64];
    }
  }
  __builtin_amdgcn_sched_barrier(0);
#pragma unroll
  for (int ks = 0; ks < NKS; ++ks) {
#pragma unroll
    for (int c = 0; c < 5; ++c)
#pragma unroll
      for (int rg = 0; rg < 4; ++rg) acc[c][rg] = mfma16(wr[ks % PF][c], ar[ks % PF][rg], acc[c][rg]);
    if (ks + PF < NKS) {
#pragma unroll
      for (int rg = 0; rg < 4; ++rg) ar[ks % PF][rg] = ap[((ks + PF) * 4 + rg) * 64];
#pragma unroll
      for (int c = 0; c < 5; ++c) wr[ks % PF][c] = wp[((ks + PF) * 5 + c) * 64];
    }
    __builtin_amdgcn_sched_barrier(0);
  }
}

template <int KSTOT>
__global__ __launch_bounds__(256, 1) void k_proj(const unsigned char* __restrict__ wt, const unsigned char* __restrict__ at,
                                                 const unsigned short* __restrict__ xin, unsigned short* __restrict__ xout) {
  extern __shared__ __attribute__((aligned(16))) unsigned char lds[];
  const int b = blockIdx.x, tid = threadIdx.x, lane = tid & 63;
  const int wave = __builtin_amdgcn_readfirstlane(tid >> 6);
  const int mt = (b & 7) * 4 + ((b >> 3) >> 3), n = (b >> 3) & 7;
  f32x4 acc[5][4];
#pragma unroll
  for (int c = 0; c < 5; ++c)
#pragma unroll
    for (int rg = 0; rg < 4; ++rg) acc[c][rg] = f32x4{0.f, 0.f, 0.f, 0.f};

  if (KSTOT == 20) {
    const int ks0 = wave * 5;
    const u32x4* wp = (const u32x4*)wt + (size_t)((n * 20 + ks0) * 5) * 64 + lane;
    const u32x4* ap = (const u32x4*)at + (size_t)((mt * 20 + ks0) * 4) * 64 + lane;
    splitk_body<5, 5>(wp, ap, acc);
  } else {
    const int ks0 = wave * 13 + (wave < 2 ? wave : 2);
    const u32x4* wp = (const u32x4*)wt + (size_t)((n * 54 + ks0) * 5) * 64 + lane;
    const u32x4* ap = (const u32x4*)at + (size_t)((mt * 54 + ks0) * 4) * 64 + lane;
    if (wave < 2)
      splitk_body<14, 7>(wp, ap, acc);
    else
      splitk_body<13, 7>(wp, ap, acc);
  }
  {
    f32x4* dst = (f32x4*)lds + wave * 1280 + lane;
#pragma unroll
    for (int c = 0; c < 5; ++c)
#pragma unroll
      for (int rg = 0; rg < 4; ++rg) dst[(c * 4 + rg) * 64] = acc[c][rg];
  }
  __syncthreads();
#pragma unroll
  for (int i = 0; i < 5; ++i) {
    int item = i * 256 + tid;
    int tile = item >> 6;
    int c = tile >> 2, rg = tile & 3;
    const f32x4* src = (const f32x4*)lds + item;
    f32x4 s = src[0] + src[1280] + src[2560] + src[3840];
    size_t off = (size_t)(mt * 64 + rg * 16 + (lane & 15)) * D_ + n * 80 + c * 16 + 4 * (lane >> 4);
    u32x2 r = *(const u32x2*)(xin + off);
    u32x2 o;
    o[0] = pack2(lo2f(r[0]) + rbf(s[0]), hi2f(r[0]) + rbf(s[1]));
    o[1] = pack2(lo2f(r[1]) + rbf(s[2]), hi2f(r[1]) + rbf(s[3]));
    *(u32x2*)(xout + off) = o;
  }
}

constexpr int K4_PF = 8;
__device__ __forceinline__ float geglu(float g, float u) {
  g = rbf(g);
  u = rbf(u);
  float y = 0.7978845608028654f * (g + 0.044715f * g * g * g);
  float ge = g / (1.f + __expf(-2.f * y));
  return rbf(ge) * u;
}

__global__ __launch_bounds__(512, 2) void k_mlp_up(const unsigned short* __restrict__ x, unsigned char* __restrict__ ws) {
  extern __shared__ __attribute__((aligned(16))) unsigned char lds[];
  const int b = blockIdx.x, lane = threadIdx.x & 63;
  const int wave = __builtin_amdgcn_readfirstlane(threadIdx.x >> 6);
  const int mt = (b & 7) * 2 + ((b >> 3) >> 4), nt = (b >> 3) & 15;
  const int first = nt * 6 + (nt < 12 ? nt : 12), cnt = nt < 12 ? 7 : 6;
  const int tok = lane & 15, q = lane >> 4;
  const bool active = wave < cnt;
  const int c = first + (active ? wave : 0);

  u32x4 xv[4][5];
  ln_load(x, mt * 128 + wave * 16, lane, xv);
  const u32x4* wp = (const u32x4*)(ws + WGU_T) + (size_t)(c * 40) * 64 + lane;
  u32x4 wr[K4_PF][2];
  if (active) {
#pragma unroll
    for (int s = 0; s < K4_PF; ++s)
#pragma unroll
      for (int i = 0; i < 2; ++i) wr[s][i] = wp[(s * 2 + i) * 64];
  }
  __builtin_amdgcn_sched_barrier(0);
  ln_store(lds, wave * 16, lane, xv);
  __syncthreads();
  if (!active) return;

  f32x4 accg[8], accu[8];
#pragma unroll
  for (int rg = 0; rg < 8; ++rg) {
    accg[rg] = f32x4{0.f, 0.f, 0.f, 0.f};
    accu[rg] = f32x4{0.f, 0.f, 0.f, 0.f};
  }
  const unsigned char* abase = lds + tok * 1280;
  int aoff[4];
#pragma unroll
  for (int m = 0; m < 4; ++m) aoff[m] = ((m << 2) ^ (q ^ tok)) << 4;
#pragma unroll
  for (int ks = 0; ks < 20; ++ks) {
#pragma unroll
    for (int rg = 0; rg < 8; ++rg) {
      u32x4 a = *(const u32x4*)(abase + aoff[ks & 3] + (ks >> 2) * 256 + rg * 16 * 1280);
      accg[rg] = mfma16(wr[ks % K4_PF][0], a, accg[rg]);
      accu[rg] = mfma16(wr[ks % K4_PF][1], a, accu[rg]);
    }
    if (ks + K4_PF < 20) {
#pragma unroll
      for (int i = 0; i < 2; ++i) wr[ks % K4_PF][i] = wp[((ks + K4_PF) * 2 + i) * 64];
    }
    __builtin_amdgcn_sched_barrier(0);
  }
#pragma unroll
  for (int rg = 0; rg < 8; ++rg) {
    u32x2 o;
    o[0] = pack2(geglu(accg[rg][0], accu[rg][0]), geglu(accg[rg][1], accu[rg][1]));
    o[1] = pack2(geglu(accg[rg][2], accu[rg][2]), geglu(accg[rg][3], accu[rg][3]));
    int mt64 = mt * 2 + (rg >> 2);
    unsigned char* dst = ws + H_T + (((size_t)mt64 * 54 + (c >> 1)) * 4 + (rg & 3)) * 1024 + lane * 16 + (c & 1) * 8;
    *(u32x2*)dst = o;
  }
}

}

extern "C" void kernel_launch(void* const* d_in, const int* in_sizes, int n_in, void* d_out, int out_size, void* d_ws,
                              size_t ws_size, hipStream_t stream) {
  const unsigned short* x0 = (const unsigned short*)d_in[0];
  const unsigned short* wqkv = (const unsigned short*)d_in[1];
  const unsigned short* wphi = (const unsigned short*)d_in[2];
  const unsigned short* wo = (const unsigned short*)d_in[3];
  const unsigned short* wg = (const unsigned short*)d_in[5];
  const unsigned short* wu = (const unsigned short*)d_in[6];
  const unsigned short* wd = (const unsigned short*)d_in[7];
  unsigned char* ws = (unsigned char*)d_ws;
  unsigned short* out = (unsigned short*)d_out;

  (void)hipFuncSetAttribute((const void*)k_qkv, hipFuncAttributeMaxDynamicSharedMemorySize, K1_LDS);
  (void)hipFuncSetAttribute((const void*)k_mlp_up, hipFuncAttributeMaxDynamicSharedMemorySize, 128 * 1280);
  (void)hipFuncSetAttribute((const void*)k_proj<20>, hipFuncAttributeMaxDynamicSharedMemorySize, 81920);
  (void)hipFuncSetAttribute((const void*)k_proj<54>, hipFuncAttributeMaxDynamicSharedMemorySize, 81920);

  k_prep<<<(N_PIECE + 255) / 256, 256, 0, stream>>>(wqkv, wphi, wo, wg, wu, wd, ws);
  unsigned short* xa = (unsigned short*)(ws + XA);
  unsigned short* xb = (unsigned short*)(ws + XB);
  const unsigned short* xin = x0;
  for (int layer = 0; layer < 2; ++layer) {
    unsigned short* xmid = xa;
    unsigned short* xend = layer == 0 ? xb : out;
    k_qkv<<<256, 256, K1_LDS, stream>>>(xin, ws);
    k_attn_out<<<256, 256, 0, stream>>>(ws);
    k_proj<20><<<256, 256, 81920, stream>>>(ws + WO_T, ws + O_T, xin, xmid);
    k_mlp_up<<<256, 512, 128 * 1280, stream>>>(xmid, ws);
    k_proj<54><<<256, 256, 81920, stream>>>(ws + WD_T, ws + H_T, xmid, xend);
    xin = xend;
  }
}
```

```cpp
#include <hip/hip_runtime.h>
#include <hip/hip_bf16.h>
#include <stdint.h>

typedef __attribute__((ext_vector_type(8))) short bf16x8;
typedef __attribute__((ext_vector_type(4))) float f32x4;
typedef __attribute__((ext_vector_type(4))) unsigned u32x4;
typedef __attribute__((ext_vector_type(2))) unsigned u32x2;
typedef __attribute__((ext_vector_type(2))) __bf16 bf16x2_t;

#define STAMP(ws, kid, slot) \
  do {                       \
  } while (0)

namespace {

constexpr int S_ = 2048, D_ = 640, H_ = 32, DF_ = 1728;

constexpr size_t WQKV_T = 0;
constexpr size_t WQKV_B = (size_t)8 * 3 * 20 * 5 * 1024;
constexpr size_t WO_T = WQKV_T + WQKV_B;
constexpr size_t WO_B = (size_t)8 * 20 * 5 * 1024;
constexpr size_t WGU_T = WO_T + WO_B;
constexpr size_t WGU_B = (size_t)108 * 20 * 2 * 1024;
constexpr size_t WD_T = WGU_T + WGU_B;
constexpr size_t WD_B = (size_t)8 * 54 * 5 * 1024;
constexpr size_t PHIQ = WD_T + WD_B;
constexpr size_t PHIQ_B = (size_t)32 * 128 * 1280;
constexpr size_t KVP = PHIQ + PHIQ_B;
constexpr size_t KVP_B = (size_t)32 * 32 * 960 * 4;
constexpr size_t O_T = KVP + KVP_B;
constexpr size_t O_B = (size_t)S_ * D_ * 2;
constexpr size_t H_T = O_T + O_B;
constexpr size_t H_B = (size_t)S_ * DF_ * 2;
constexpr size_t XA = H_T + H_B;
constexpr size_t XB = XA + O_B;
constexpr size_t WS_END = XB + O_B;

constexpr int N_PIECE_QKV = 8 * 3 * 20 * 5 * 64;
constexpr int N_PIECE_WO = 8 * 20 * 5 * 64;
constexpr int N_PIECE_GU = 108 * 20 * 2 * 64;
constexpr int N_PIECE_WD = 8 * 54 * 5 * 64;
constexpr int N_PIECE = N_PIECE_QKV + N_PIECE_WO + N_PIECE_GU + N_PIECE_WD;

__device__ __forceinline__ unsigned pack2(float a, float b) {
  bf16x2_t v;
  v[0] = (__bf16)a;
  v[1] = (__bf16)b;
  return __builtin_bit_cast(unsigned, v);
}
__device__ __forceinline__ float lo2f(unsigned u) { return __uint_as_float(u << 16); }
__device__ __forceinline__ float hi2f(unsigned u) { return __uint_as_float(u & 0xffff0000u); }
__device__ __forceinline__ float rbf(float a) {
  bf16x2_t v;
  v[0] = (__bf16)a;
  v[1] = (__bf16)0.f;
  return lo2f(__builtin_bit_cast(unsigned, v));
}
__device__ __forceinline__ float us2f(unsigned short u) { return __uint_as_float((unsigned)u << 16); }

__device__ __forceinline__ f32x4 mfma16(u32x4 a, u32x4 b, f32x4 c) {
  return __builtin_amdgcn_mfma_f32_16x16x32_bf16(__builtin_bit_cast(bf16x8, a), __builtin_bit_cast(bf16x8, b), c, 0, 0,
                                                 0);
}

typedef __attribute__((ext_vector_type(2))) float f32x2;

constexpr int PB_QKV = 8 * 3 * 20, PB_WO = 8 * 20, PB_GU = 27 * 2 * 20, PB_WD = 8 * 54;
constexpr int PB_TOT = PB_QKV + PB_WO + PB_GU + PB_WD;

__global__ __launch_bounds__(256) void k_prep(const unsigned short* __restrict__ wqkv,
                                              const unsigned short* __restrict__ wphi,
                                              const unsigned short* __restrict__ wo,
                                              const unsigned short* __restrict__ wg,
                                              const unsigned short* __restrict__ wu,
                                              const unsigned short* __restrict__ wd, unsigned char* __restrict__ ws) {
  __shared__ __attribute__((aligned(16))) unsigned short tile[32 * 88];
  __shared__ float ph[4 * 400];
  const int tid = threadIdx.x;
  int blk = blockIdx.x;
  const int row = tid >> 3, seg = tid & 7;
  const int rq = row >> 3, rh = (row >> 2) & 1, rr = row & 3;
  const unsigned short* src;
  int ncg, fold = 0, head0 = 0;
  u32x4* dst;
  if (blk < PB_QKV) {
    int ks = blk % 20, t = (blk / 20) % 3, g = blk / 60;
    src = wqkv + (size_t)(ks * 32 + row) * 1920 + t * 640 + g * 80;
    ncg = 5;
    fold = t < 2;
    head0 = g * 4;
    dst = (u32x4*)(ws + WQKV_T) + (size_t)blk * 320;
  } else if (blk < PB_QKV + PB_WO) {
    blk -= PB_QKV;
    int ks = blk % 20, n = blk / 20;
    int head, d;
    if (ks < 16) {
      head = 2 * ks + rh;
      d = 4 * rq + rr;
    } else {
      head = 8 * (ks - 16) + 2 * rq + rh;
      d = 16 + rr;
    }
    src = wo + (size_t)(head * 20 + d) * 640 + n * 80;
    ncg = 5;
    dst = (u32x4*)(ws + WO_T) + (size_t)blk * 320;
  } else if (blk < PB_QKV + PB_WO + PB_GU) {
    blk -= PB_QKV + PB_WO;
    int ks = blk % 20, gu = (blk / 20) & 1, cb = blk / 40;
    src = (gu ? wu : wg) + (size_t)(ks * 32 + row) * DF_ + cb * 64;
    ncg = 4;
    dst = (u32x4*)(ws + WGU_T) + (size_t)((cb * 4 * 20 + ks) * 2 + gu) * 64;
  } else {
    blk -= PB_QKV + PB_WO + PB_GU;
    int ks = blk % 54, n = blk / 54;
    int feat = (2 * ks + rh) * 16 + 4 * rq + rr;
    src = wd + (size_t)feat * 640 + n * 80;
    ncg = 5;
    dst = (u32x4*)(ws + WD_T) + (size_t)blk * 320;
  }
  u32x4 v0 = {0u, 0u, 0u, 0u}, v1 = {0u, 0u, 0u, 0u};
  v0 = *(const u32x4*)(src + seg * 8);
  if (seg < 2 && ncg == 5) v1 = *(const u32x4*)(src + 64 + seg * 8);
  if (fold) {
    for (int e = tid; e < 1600; e += 256) ph[e] = us2f(wphi[head0 * 400 + e]);
  }
  *(u32x4*)(tile + row * 88 + seg * 8) = v0;
  if (seg < 2) *(u32x4*)(tile + row * 88 + 64 + seg * 8) = v1;
  __syncthreads();
  const int cgstride = (ncg == 4) ? 40 * 64 : 64;
#pragma unroll
  for (int it = 0; it < 2; ++it) {
    int p = it * 256 + tid;
    if (p < ncg * 64) {
      int cg = p >> 6, lane = p & 63, i = lane & 15, q = lane >> 4;
      float v[8];
      if (fold) {
        int hh = i >> 2, f = cg * 4 + (i & 3);
        float pw[20];
#pragma unroll
        for (int d = 0; d < 20; ++d) pw[d] = ph[hh * 400 + d * 20 + f];
#pragma unroll
        for (int j = 0; j < 8; ++j) {
          const unsigned short* tr = tile + (8 * q + j) * 88 + hh * 20;
          float a = 0.f;
#pragma unroll
          for (int d = 0; d < 20; ++d) a = fmaf(us2f(tr[d]), pw[d], a);
          v[j] = a;
        }
      } else {
        int col = (blockIdx.x < PB_QKV) ? ((i >> 2) * 20 + cg * 4 + (i & 3)) : (cg * 16 + i);
#pragma unroll
        for (int j = 0; j < 8; ++j) v[j] = us2f(tile[(8 * q + j) * 88 + col]);
      }
      u32x4 o;
      o[0] = pack2(v[0], v[1]);
      o[1] = pack2(v[2], v[3]);
      o[2] = pack2(v[4], v[5]);
      o[3] = pack2(v[6], v[7]);
      dst[cg * cgstride + lane] = o;
    }
  }
}

template <int NP>
__device__ __forceinline__ void ln_load(const unsigned short* __restrict__ x, int grow0, int lane, u32x4 (&v)[NP][5]) {
  int sub = lane >> 4, c16 = lane & 15;
#pragma unroll
  for (int p = 0; p < NP; ++p) {
    const u32x4* src = (const u32x4*)(x + (size_t)(grow0 + p * 4 + sub) * D_) + c16;
#pragma unroll
    for (int i = 0; i < 5; ++i) v[p][i] = src[16 * i];
  }
}
__device__ __forceinline__ float red16(float s) {
  s += __shfl_xor(s, 1);
  s += __shfl_xor(s, 2);
  s += __shfl_xor(s, 4);
  s += __shfl_xor(s, 8);
  return s;
}
template <int NP>
__device__ __forceinline__ void ln_store(unsigned char* lds, int trow0, int lane, u32x4 (&v)[NP][5]) {
  int sub = lane >> 4, c16 = lane & 15;
#pragma unroll
  for (int p = 0; p < NP; ++p) {
    f32x2 c[20];
    f32x2 s2 = {0.f, 0.f};
#pragma unroll
    for (int i = 0; i < 5; ++i)
#pragma unroll
      for (int e = 0; e < 4; ++e) {
        c[i * 4 + e] = f32x2{lo2f(v[p][i][e]), hi2f(v[p][i][e])};
        s2 += c[i * 4 + e];
      }
    float mean = red16(s2[0] + s2[1]) * (1.f / 640.f);
    f32x2 m2 = {mean, mean};
    f32x2 q2 = {0.f, 0.f};
#pragma unroll
    for (int k = 0; k < 20; ++k) {
      c[k] -= m2;
      q2 += c[k] * c[k];
    }
    float rstd = rsqrtf(red16(q2[0] + q2[1]) * (1.f / 640.f) + 1e-5f);
    f32x2 r2 = {rstd, rstd};
    int row = trow0 + p * 4 + sub;
    unsigned char* dst = lds + row * 1280 + ((c16 ^ (row & 15)) << 4);
#pragma unroll
    for (int i = 0; i < 5; ++i) {
      u32x4 o;
#pragma unroll
      for (int e = 0; e < 4; ++e) {
        f32x2 y = c[i * 4 + e] * r2;
        o[e] = pack2(y[0], y[1]);
      }
      *(u32x4*)(dst + 256 * i) = o;
    }
  }
}

__device__ __forceinline__ void dual_softmax(const f32x4 (&acc)[5], unsigned (&out)[20]) {
  float p[20];
#pragma unroll
  for (int c = 0; c < 5; ++c) {
    unsigned u0 = pack2(acc[c][0], acc[c][1]), u1 = pack2(acc[c][2], acc[c][3]);
    p[4 * c + 0] = lo2f(u0);
    p[4 * c + 1] = hi2f(u0);
    p[4 * c + 2] = lo2f(u1);
    p[4 * c + 3] = hi2f(u1);
  }
  float mx = p[0], mn = p[0];
#pragma unroll
  for (int f = 1; f < 20; ++f) {
    mx = fmaxf(mx, p[f]);
    mn = fminf(mn, p[f]);
  }
  const float L2E = 1.4426950408889634f;
  float e[20], n[20], s1 = 0.f, s2 = 0.f;
  float mxl = -mx * L2E, mnl = mn * L2E;
#pragma unroll
  for (int f = 0; f < 20; ++f) {
    e[f] = __builtin_amdgcn_exp2f(fmaf(p[f], L2E, mxl));
    n[f] = __builtin_amdgcn_exp2f(fmaf(p[f], -L2E, mnl));
    s1 += e[f];
    s2 += n[f];
  }
  float r1 = 1.f / s1, r2 = 1.f / s2;
#pragma unroll
  for (int f = 0; f < 10; ++f) {
    out[f] = pack2(e[2 * f] * r1, e[2 * f + 1] * r1);
    out[10 + f] = pack2(n[2 * f] * r2, n[2 * f + 1] * r2);
  }
}

constexpr int K1_RED = 0;
constexpr int K1_PHI_OFF = 6 * 20480;
constexpr int K1_V_OFF = K1_PHI_OFF + 4 * 40 * 144;
constexpr int K1_LDS = K1_V_OFF + 4 * 24 * 144 + 2048;
constexpr int K1_PF = 2;

__global__ __launch_bounds__(768, 3) void k_qkv(const unsigned short* __restrict__ x, unsigned char* __restrict__ ws) {
  extern __shared__ __attribute__((aligned(16))) unsigned char lds[];
  const int b = blockIdx.x, lane = threadIdx.x & 63;
  const int wave = __builtin_amdgcn_readfirstlane(threadIdx.x >> 6);
  const int xcd = b & 7, idx = b >> 3;
  const int mt = (xcd >> 1) * 8 + (idx >> 2), g = (xcd & 1) * 4 + (idx & 3);
  const int tok = lane & 15, q = lane >> 4;
  const int t = wave % 3, kq = wave / 3;

  STAMP(ws, 1, 0);
  u32x4 xv[2][5];
  if (wave < 8) ln_load<2>(x, mt * 64 + wave * 8, lane, xv);
  const u32x4* wp = (const u32x4*)(ws + WQKV_T) + (size_t)((g * 3 + t) * 100 + kq * 25) * 64 + lane;
  u32x4 wr[K1_PF][5];
#pragma unroll
  for (int s = 0; s < K1_PF; ++s)
#pragma unroll
    for (int c = 0; c < 5; ++c) wr[s][c] = wp[(s * 5 + c) * 64];
  __builtin_amdgcn_sched_barrier(0);
  if (wave < 8) ln_store<2>(lds, wave * 8, lane, xv);
  STAMP(ws, 1, 1);
  __syncthreads();
  STAMP(ws, 1, 2);

  f32x4 acc[4][5];
#pragma unroll
  for (int rg = 0; rg < 4; ++rg)
#pragma unroll
    for (int c = 0; c < 5; ++c) acc[rg][c] = f32x4{0.f, 0.f, 0.f, 0.f};
  {
    const unsigned char* abase = lds + tok * 1280;
#pragma unroll
    for (int s = 0; s < 5; ++s) {
      int ks = kq * 5 + s;
      int off = (ks >> 2) * 256 + (((((ks & 3) << 2) | q) ^ tok) << 4);
      u32x4 a[4];
#pragma unroll
      for (int rg = 0; rg < 4; ++rg) a[rg] = *(const u32x4*)(abase + off + rg * 16 * 1280);
#pragma unroll
      for (int c = 0; c < 5; ++c)
#pragma unroll
        for (int rg = 0; rg < 4; ++rg) acc[rg][c] = mfma16(wr[s % K1_PF][c], a[rg], acc[rg][c]);
      if (s + K1_PF < 5) {
#pragma unroll
        for (int c = 0; c < 5; ++c) wr[s % K1_PF][c] = wp[((s + K1_PF) * 5 + c) * 64];
      }
      __builtin_amdgcn_sched_barrier(0);
    }
  }
  STAMP(ws, 1, 3);
  __syncthreads();
  f32x4* red = (f32x4*)(lds + K1_RED) + lane;
  if (kq >= 2) {
    f32x4* d = red + ((kq - 2) * 3 + t) * 1280;
#pragma unroll
    for (int rg = 0; rg < 4; ++rg)
#pragma unroll
      for (int c = 0; c < 5; ++c) d[(rg * 5 + c) * 64] = acc[rg][c];
  }
  __syncthreads();
  if (kq < 2) {
    f32x4* d = red + (kq * 3 + t) * 1280;
#pragma unroll
    for (int rg = 0; rg < 4; ++rg)
#pragma unroll
      for (int c = 0; c < 5; ++c) d[(rg * 5 + c) * 64] += acc[rg][c];
  }
  __syncthreads();
  STAMP(ws, 1, 4);
  {
    const int rg = kq;
    f32x4 p[5];
#pragma unroll
    for (int c = 0; c < 5; ++c) p[c] = red[t * 1280 + (rg * 5 + c) * 64] + red[(3 + t) * 1280 + (rg * 5 + c) * 64];
    if (t == 0) {
      const int head = g * 4 + q;
      unsigned o[20];
      dual_softmax(p, o);
      u32x4* dst = (u32x4*)(ws + PHIQ + ((size_t)head * 128 + mt * 4 + rg) * 1280) + tok;
#pragma unroll
      for (int c = 0; c < 5; ++c) dst[16 * c] = u32x4{o[4 * c], o[4 * c + 1], o[4 * c + 2], o[4 * c + 3]};
    } else if (t == 1) {
      unsigned o[20];
      dual_softmax(p, o);
      unsigned short* dst = (unsigned short*)(lds + K1_PHI_OFF + q * (40 * 144) + (rg * 16 + tok) * 2);
#pragma unroll
      for (int f = 0; f < 20; ++f) {
        dst[(2 * f) * 72] = (unsigned short)(o[f] & 0xffffu);
        dst[(2 * f + 1) * 72] = (unsigned short)(o[f] >> 16);
      }
    } else {
      unsigned short* dst = (unsigned short*)(lds + K1_V_OFF + q * (24 * 144) + (rg * 16 + tok) * 2);
#pragma unroll
      for (int c = 0; c < 5; ++c) {
        unsigned p0 = pack2(p[c][0], p[c][1]), p1 = pack2(p[c][2], p[c][3]);
        dst[(4 * c + 0) * 72] = (unsigned short)(p0 & 0xffffu);
        dst[(4 * c + 1) * 72] = (unsigned short)(p0 >> 16);
        dst[(4 * c + 2) * 72] = (unsigned short)(p1 & 0xffffu);
        dst[(4 * c + 3) * 72] = (unsigned short)(p1 >> 16);
      }
      dst[20 * 72] = 0x3f80;
      dst[21 * 72] = 0;
      dst[22 * 72] = 0;
      dst[23 * 72] = 0;
    }
  }
  STAMP(ws, 1, 5);
  __syncthreads();
  {
    const int hh = kq, ft = t;
    const unsigned char* vb = lds + K1_V_OFF + hh * (24 * 144) + tok * 144 + q * 16;
    const unsigned char* pb = lds + K1_PHI_OFF + hh * (40 * 144) + (ft * 16 + tok) * 144 + q * 16;
    f32x4 d0 = {0.f, 0.f, 0.f, 0.f}, d1 = {0.f, 0.f, 0.f, 0.f};
#pragma unroll
    for (int ks = 0; ks < 2; ++ks) {
      u32x4 va0 = *(const u32x4*)(vb + ks * 64);
      u32x4 va1 = *(const u32x4*)(vb + 16 * 144 + ks * 64);
      u32x4 pf = *(const u32x4*)(pb + ks * 64);
      d0 = mfma16(va0, pf, d0);
      d1 = mfma16(va1, pf, d1);
    }
    float* dst = (float*)(ws + KVP) + ((size_t)mt * 32 + g * 4 + hh) * 960;
    int f = ft * 16 + tok;
    if (f < 40) {
      *(f32x4*)(dst + f * 24 + 4 * q) = d0;
      if (q < 2) *(f32x4*)(dst + f * 24 + 16 + 4 * q) = d1;
    }
  }
  STAMP(ws, 1, 6);
}

__global__ __launch_bounds__(512) void k_attn_out(unsigned char* __restrict__ ws) {
  __shared__ __attribute__((aligned(16))) unsigned short sfr[4 * 64 * 8];
  __shared__ __attribute__((aligned(16))) float shalf[240 * 4];
  const int b = blockIdx.x, tid = threadIdx.x, lane = tid & 63;
  const int wave = __builtin_amdgcn_readfirstlane(tid >> 6);
  const int xcd = b & 7, idx = b >> 3;
  const int head = (xcd & 1) * 16 + (xcd >> 1) * 4 + (idx >> 3), mt256 = idx & 7;
  const int tok = lane & 15, q = lane >> 4;

  STAMP(ws, 2, 0);
  const int e4 = tid < 240 ? tid : (tid < 480 ? tid - 240 : 0), ph = tid < 240 ? 0 : 1;
  f32x4 part[16];
  {
    const f32x4* src = (const f32x4*)(ws + KVP) + ((size_t)ph * 16 * 32 + head) * 240 + e4;
#pragma unroll
    for (int p = 0; p < 16; ++p) part[p] = src[(size_t)p * 32 * 240];
  }
  u32x4 pq0[2], pq1[2];
#pragma unroll
  for (int i = 0; i < 2; ++i) {
    int rgG = mt256 * 16 + wave * 2 + i;
    const u32x4* src = (const u32x4*)(ws + PHIQ + ((size_t)head * 128 + rgG) * 1280);
    pq0[i] = src[lane];
    pq1[i] = src[64 + tok];
  }
  __builtin_amdgcn_sched_barrier(0);
  if (tid < 256) *(u32x4*)(sfr + tid * 8) = u32x4{0u, 0u, 0u, 0u};
  f32x4 s = part[0];
#pragma unroll
  for (int p = 1; p < 16; ++p) s += part[p];
  if (tid >= 240 && tid < 480) *(f32x4*)(shalf + e4 * 4) = s;
  STAMP(ws, 2, 1);
  __syncthreads();
  if (tid < 240) {
    s += *(const f32x4*)(shalf + e4 * 4);
    int f = tid / 6, d0 = (tid % 6) * 4;
    unsigned u0 = pack2(s[0], s[1]), u1 = pack2(s[2], s[3]);
    unsigned short* dst = sfr + (((d0 >> 4) * 2 + (f >> 5)) * 64 + ((f & 31) >> 3) * 16 + (d0 & 15)) * 8 + (f & 7);
    dst[0] = (unsigned short)(u0 & 0xffffu);
    dst[8] = (unsigned short)(u0 >> 16);
    dst[16] = (unsigned short)(u1 & 0xffffu);
    dst[24] = (unsigned short)(u1 >> 16);
  }
#pragma unroll
  for (int i = 0; i < 2; ++i)
#pragma unroll
    for (int e = 0; e < 4; ++e) pq1[i][e] = q == 0 ? pq1[i][e] : 0u;
  __syncthreads();
  STAMP(ws, 2, 2);
  u32x4 ka[2][2];
#pragma unroll
  for (int dt = 0; dt < 2; ++dt)
#pragma unroll
    for (int ks = 0; ks < 2; ++ks) ka[dt][ks] = *(const u32x4*)(sfr + ((dt * 2 + ks) * 64 + lane) * 8);
#pragma unroll
  for (int i = 0; i < 2; ++i) {
    f32x4 d0 = {0.f, 0.f, 0.f, 0.f}, d1 = {0.f, 0.f, 0.f, 0.f};
    d0 = mfma16(ka[0][0], pq0[i], d0);
    d0 = mfma16(ka[0][1], pq1[i], d0);
    d1 = mfma16(ka[1][0], pq0[i], d1);
    d1 = mfma16(ka[1][1], pq1[i], d1);
    float den = __shfl(d1[0], 16 + tok);
    den = rbf(rbf(den) + 1e-6f);
    float rd = 1.f / den;
    int rgG = mt256 * 16 + wave * 2 + i;
    int mt64 = rgG >> 2, rg = rgG & 3;
    {
      u32x2 o;
      o[0] = pack2(rbf(d0[0]) * rd, rbf(d0[1]) * rd);
      o[1] = pack2(rbf(d0[2]) * rd, rbf(d0[3]) * rd);
      unsigned char* dst = ws + O_T + (((size_t)mt64 * 20 + (head >> 1)) * 4 + rg) * 1024 + lane * 16 + (head & 1) * 8;
      *(u32x2*)dst = o;
    }
    if (q == 0) {
      u32x2 o;
      o[0] = pack2(rbf(d1[0]) * rd, rbf(d1[1]) * rd);
      o[1] = pack2(rbf(d1[2]) * rd, rbf(d1[3]) * rd);
      unsigned char* dst = ws + O_T + (((size_t)mt64 * 20 + 16 + (head >> 3)) * 4 + rg) * 1024 +
                           (((head & 7) >> 1) * 16 + tok) * 16 + (head & 1) * 8;
      *(u32x2*)dst = o;
    }
  }
  STAMP(ws, 2, 3);
}

template <int NKS, int PF>
__device__ __forceinline__ void splitk_body(const u32x4* __restrict__ wp, const u32x4* __restrict__ ap,
                                            f32x4 (&acc)[5][4]) {
  u32x4 wr[PF][5], ar[PF][4];
#pragma unroll
  for (int s = 0; s < PF; ++s) {
    if (s < NKS) {
#pragma unroll
      for (int rg = 0; rg < 4; ++rg) ar[s][rg] = ap[(s * 4 + rg) * 64];
#pragma unroll
      for (int c = 0; c < 5; ++c) wr[s][c] = wp[(s * 5 + c) * 64];
    }
  }
  __builtin_amdgcn_sched_barrier(0);
#pragma unroll
  for (int ks = 0; ks < NKS; ++ks) {
#pragma unroll
    for (int c = 0; c < 5; ++c)
#pragma unroll
      for (int rg = 0; rg < 4; ++rg) acc[c][rg] = mfma16(wr[ks % PF][c], ar[ks % PF][rg], acc[c][rg]);
    if (ks + PF < NKS) {
#pragma unroll
      for (int rg = 0; rg < 4; ++rg) ar[ks % PF][rg] = ap[((ks + PF) * 4 + rg) * 64];
#pragma unroll
      for (int c = 0; c < 5; ++c) wr[ks % PF][c] = wp[((ks + PF) * 5 + c) * 64];
    }
    __builtin_amdgcn_sched_barrier(0);
  }
}

template <int KSTOT>
__global__ __launch_bounds__(512, 2) void k_proj(unsigned char* __restrict__ ws, const unsigned short* __restrict__ xin,
                                                 unsigned short* __restrict__ xout) {
  const unsigned char* wt = ws + (KSTOT == 20 ? WO_T : WD_T);
  const unsigned char* at = ws + (KSTOT == 20 ? O_T : H_T);
  extern __shared__ __attribute__((aligned(16))) unsigned char lds[];
  const int b = blockIdx.x, tid = threadIdx.x, lane = tid & 63;
  const int wave = __builtin_amdgcn_readfirstlane(tid >> 6);
  const int xcd = b & 7, idx = b >> 3;
  const int mt = (xcd >> 1) * 8 + (idx >> 2), n = (xcd & 1) * 4 + (idx & 3);
  STAMP(ws, KSTOT == 20 ? 3 : 5, 0);
  f32x4 acc[5][4];
#pragma unroll
  for (int c = 0; c < 5; ++c)
#pragma unroll
    for (int rg = 0; rg < 4; ++rg) acc[c][rg] = f32x4{0.f, 0.f, 0.f, 0.f};
  u32x2 res[3];
#pragma unroll
  for (int i = 0; i < 3; ++i) {
    int tile = i * 8 + wave;
    tile = tile < 20 ? tile : 0;
    int c = tile >> 2, rg = tile & 3;
    size_t off = (size_t)(mt * 64 + rg * 16 + (lane & 15)) * D_ + n * 80 + c * 16 + 4 * (lane >> 4);
    res[i] = *(const u32x2*)(xin + off);
  }

  if (KSTOT == 20) {
    const int ks0 = wave * 2 + (wave < 4 ? wave : 4);
    const u32x4* wp = (const u32x4*)wt + (size_t)((n * 20 + ks0) * 5) * 64 + lane;
    const u32x4* ap = (const u32x4*)at + (size_t)((mt * 20 + ks0) * 4) * 64 + lane;
    if (wave < 4)
      splitk_body<3, 3>(wp, ap, acc);
    else
      splitk_body<2, 2>(wp, ap, acc);
  } else {
    const int ks0 = wave * 6 + (wave < 6 ? wave : 6);
    const u32x4* wp = (const u32x4*)wt + (size_t)((n * 54 + ks0) * 5) * 64 + lane;
    const u32x4* ap = (const u32x4*)at + (size_t)((mt * 54 + ks0) * 4) * 64 + lane;
    if (wave < 6)
      splitk_body<7, 3>(wp, ap, acc);
    else
      splitk_body<6, 3>(wp, ap, acc);
  }
  STAMP(ws, KSTOT == 20 ? 3 : 5, 1);
  f32x4* reg = (f32x4*)lds + (wave & 3) * 1280 + lane;
  if (wave >= 4) {
#pragma unroll
    for (int c = 0; c < 5; ++c)
#pragma unroll
      for (int rg = 0; rg < 4; ++rg) reg[(c * 4 + rg) * 64] = acc[c][rg];
  }
  __syncthreads();
  if (wave < 4) {
#pragma unroll
    for (int c = 0; c < 5; ++c)
#pragma unroll
      for (int rg = 0; rg < 4; ++rg) reg[(c * 4 + rg) * 64] += acc[c][rg];
  }
  __syncthreads();
  STAMP(ws, KSTOT == 20 ? 3 : 5, 2);
#pragma unroll
  for (int i = 0; i < 3; ++i) {
    int tile = i * 8 + wave;
    if (tile < 20) {
      int c = tile >> 2, rg = tile & 3;
      const f32x4* src = (const f32x4*)lds + tile * 64 + lane;
      f32x4 s = (src[0] + src[1280]) + (src[2560] + src[3840]);
      size_t off = (size_t)(mt * 64 + rg * 16 + (lane & 15)) * D_ + n * 80 + c * 16 + 4 * (lane >> 4);
      u32x2 r = res[i];
      u32x2 o;
      o[0] = pack2(lo2f(r[0]) + rbf(s[0]), hi2f(r[0]) + rbf(s[1]));
      o[1] = pack2(lo2f(r[1]) + rbf(s[2]), hi2f(r[1]) + rbf(s[3]));
      *(u32x2*)(xout + off) = o;
    }
  }
  STAMP(ws, KSTOT == 20 ? 3 : 5, 3);
}

constexpr int K4_PF = 8;
__device__ __forceinline__ unsigned geglu2(float g0, float g1, float u0, float u1) {
  unsigned gp = pack2(g0, g1), up = pack2(u0, u1);
  f32x2 g = {lo2f(gp), hi2f(gp)}, u = {lo2f(up), hi2f(up)};
  const float A = -2.302208198f, B = -0.1029432397f;
  f32x2 g2 = g * g;
  f32x2 w = g * (f32x2{B, B} * g2 + f32x2{A, A});
  f32x2 d = {1.f + __builtin_amdgcn_exp2f(w[0]), 1.f + __builtin_amdgcn_exp2f(w[1])};
  f32x2 ge = {g[0] * __builtin_amdgcn_rcpf(d[0]), g[1] * __builtin_amdgcn_rcpf(d[1])};
  unsigned gep = pack2(ge[0], ge[1]);
  f32x2 h = f32x2{lo2f(gep), hi2f(gep)} * u;
  return pack2(h[0], h[1]);
}

__global__ __launch_bounds__(512, 2) void k_mlp_up(const unsigned short* __restrict__ x, unsigned char* __restrict__ ws) {
  extern __shared__ __attribute__((aligned(16))) unsigned char lds[];
  const int b = blockIdx.x, lane = threadIdx.x & 63;
  const int wave = __builtin_amdgcn_readfirstlane(threadIdx.x >> 6);
  const int xcd = b & 7, idx = b >> 3;
  const int mt = (xcd >> 1) * 4 + (idx >> 3), nt = idx & 7;
  const int first = (xcd & 1) * 54 + nt * 6 + (nt < 6 ? nt : 6), cnt = nt < 6 ? 7 : 6;
  const int tok = lane & 15, q = lane >> 4;
  const bool active = wave < cnt;
  const int c = first + (active ? wave : 0);

  STAMP(ws, 4, 0);
  u32x4 xv[4][5];
  ln_load<4>(x, mt * 128 + wave * 16, lane, xv);
  const u32x4* wp = (const u32x4*)(ws + WGU_T) + (size_t)(c * 40) * 64 + lane;
  u32x4 wr[K4_PF][2];
  if (active) {
#pragma unroll
    for (int s = 0; s < K4_PF; ++s)
#pragma unroll
      for (int i = 0; i < 2; ++i) wr[s][i] = wp[(s * 2 + i) * 64];
  }
  __builtin_amdgcn_sched_barrier(0);
  ln_store<4>(lds, wave * 16, lane, xv);
  STAMP(ws, 4, 1);
  __syncthreads();
  STAMP(ws, 4, 2);
  if (!active) return;

  f32x4 accg[8], accu[8];
#pragma unroll
  for (int rg = 0; rg < 8; ++rg) {
    accg[rg] = f32x4{0.f, 0.f, 0.f, 0.f};
    accu[rg] = f32x4{0.f, 0.f, 0.f, 0.f};
  }
  const unsigned char* abase = lds + tok * 1280;
  int aoff[4];
#pragma unroll
  for (int m = 0; m < 4; ++m) aoff[m] = ((m << 2) ^ (q ^ tok)) << 4;
#pragma unroll
  for (int ks = 0; ks < 20; ++ks) {
#pragma unroll
    for (int rg = 0; rg < 8; ++rg) {
      u32x4 a = *(const u32x4*)(abase + aoff[ks & 3] + (ks >> 2) * 256 + rg * 16 * 1280);
      accg[rg] = mfma16(wr[ks % K4_PF][0], a, accg[rg]);
      accu[rg] = mfma16(wr[ks % K4_PF][1], a, accu[rg]);
    }
    if (ks + K4_PF < 20) {
#pragma unroll
      for (int i = 0; i < 2; ++i) wr[ks % K4_PF][i] = wp[((ks + K4_PF) * 2 + i) * 64];
    }
    __builtin_amdgcn_sched_barrier(0);
  }
  STAMP(ws, 4, 3);
#pragma unroll
  for (int rg = 0; rg < 8; ++rg) {
    u32x2 o;
    o[0] = geglu2(accg[rg][0], accg[rg][1], accu[rg][0], accu[rg][1]);
    o[1] = geglu2(accg[rg][2], accg[rg][3], accu[rg][2], accu[rg][3]);
    int mt64 = mt * 2 + (rg >> 2);
    unsigned char* dst = ws + H_T + (((size_t)mt64 * 54 + (c >> 1)) * 4 + (rg & 3)) * 1024 + lane * 16 + (c & 1) * 8;
    *(u32x2*)dst = o;
  }
  STAMP(ws, 4, 4);
}

}

extern "C" void kernel_launch(void* const* d_in, const int* in_sizes, int n_in, void* d_out, int out_size, void* d_ws,
                              size_t ws_size, hipStream_t stream) {
  const unsigned short* x0 = (const unsigned short*)d_in[0];
  const unsigned short* wqkv = (const unsigned short*)d_in[1];
  const unsigned short* wphi = (const unsigned short*)d_in[2];
  const unsigned short* wo = (const unsigned short*)d_in[3];
  const unsigned short* wg = (const unsigned short*)d_in[5];
  const unsigned short* wu = (const unsigned short*)d_in[6];
  const unsigned short* wd = (const unsigned short*)d_in[7];
  unsigned char* ws = (unsigned char*)d_ws;
  unsigned short* out = (unsigned short*)d_out;

  (void)hipFuncSetAttribute((const void*)k_qkv, hipFuncAttributeMaxDynamicSharedMemorySize, K1_LDS);
  (void)hipFuncSetAttribute((const void*)k_mlp_up, hipFuncAttributeMaxDynamicSharedMemorySize, 128 * 1280);
  (void)hipFuncSetAttribute((const void*)k_proj<20>, hipFuncAttributeMaxDynamicSharedMemorySize, 81920);
  (void)hipFuncSetAttribute((const void*)k_proj<54>, hipFuncAttributeMaxDynamicSharedMemorySize, 81920);

  k_prep<<<PB_TOT, 256, 0, stream>>>(wqkv, wphi, wo, wg, wu, wd, ws);
  unsigned short* xa = (unsigned short*)(ws + XA);
  unsigned short* xb = (unsigned short*)(ws + XB);
  const unsigned short* xin = x0;
  for (int layer = 0; layer < 2; ++layer) {
    unsigned short* xmid = xa;
    unsigned short* xend = layer == 0 ? xb : out;
    k_qkv<<<256, 768, K1_LDS, stream>>>(xin, ws);
    k_attn_out<<<256, 512, 0, stream>>>(ws);
    k_proj<20><<<256, 512, 81920, stream>>>(ws, xin, xmid);
    k_mlp_up<<<256, 512, 128 * 1280, stream>>>(xmid, ws);
    k_proj<54><<<256, 512, 81920, stream>>>(ws, xmid, xend);
    xin = xend;
  }
}
```

```cpp
#include <hip/hip_runtime.h>
#include <hip/hip_bf16.h>
#include <stdint.h>

typedef __attribute__((ext_vector_type(8))) short bf16x8;
typedef __attribute__((ext_vector_type(4))) float f32x4;
typedef __attribute__((ext_vector_type(4))) unsigned u32x4;
typedef __attribute__((ext_vector_type(2))) unsigned u32x2;
typedef __attribute__((ext_vector_type(2))) __bf16 bf16x2_t;

#define STAMP(ws, kid, slot) \
  do {                       \
  } while (0)

namespace {

constexpr int S_ = 2048, D_ = 640, H_ = 32, DF_ = 1728;

constexpr size_t WQKV_T = 0;
constexpr size_t WQKV_B = (size_t)8 * 3 * 20 * 5 * 1024;
constexpr size_t WO_T = WQKV_T + WQKV_B;
constexpr size_t WO_B = (size_t)8 * 20 * 5 * 1024;
constexpr size_t WGU_T = WO_T + WO_B;
constexpr size_t WGU_B = (size_t)108 * 20 * 2 * 1024;
constexpr size_t WD_T = WGU_T + WGU_B;
constexpr size_t WD_B = (size_t)8 * 54 * 5 * 1024;
constexpr size_t PHIQ = WD_T + WD_B;
constexpr size_t PHIQ_B = (size_t)32 * 128 * 1280;
constexpr size_t KVP = PHIQ + PHIQ_B;
constexpr size_t KVP_B = (size_t)32 * 32 * 960 * 4;
constexpr size_t O_T = KVP + KVP_B;
constexpr size_t O_B = (size_t)S_ * D_ * 2;
constexpr size_t H_T = O_T + O_B;
constexpr size_t H_B = (size_t)S_ * DF_ * 2;
constexpr size_t XF0 = H_T + H_B;
constexpr size_t XF1 = XF0 + O_B;
constexpr size_t XF2 = XF1 + O_B;
constexpr size_t ST_B = (size_t)S_ * 40 * 8;
constexpr size_t ST0 = XF2 + O_B;
constexpr size_t ST1 = ST0 + ST_B;
constexpr size_t ST2 = ST1 + ST_B;
constexpr size_t CSQ = ST2 + ST_B;
constexpr size_t CSQ_B = (size_t)8 * 20 * 240 * 4;
constexpr size_t CSG = CSQ + CSQ_B;
constexpr size_t CSG_B = (size_t)108 * 2 * 20 * 16 * 4;
constexpr size_t WS_END = CSG + CSG_B;

typedef __attribute__((ext_vector_type(2))) float f32x2;

__device__ __forceinline__ unsigned pack2(float a, float b) {
  bf16x2_t v;
  v[0] = (__bf16)a;
  v[1] = (__bf16)b;
  return __builtin_bit_cast(unsigned, v);
}
__device__ __forceinline__ float lo2f(unsigned u) { return __uint_as_float(u << 16); }
__device__ __forceinline__ float hi2f(unsigned u) { return __uint_as_float(u & 0xffff0000u); }
__device__ __forceinline__ float rbf(float a) {
  bf16x2_t v;
  v[0] = (__bf16)a;
  v[1] = (__bf16)0.f;
  return lo2f(__builtin_bit_cast(unsigned, v));
}
__device__ __forceinline__ float us2f(unsigned short u) { return __uint_as_float((unsigned)u << 16); }

__device__ __forceinline__ f32x4 mfma16(u32x4 a, u32x4 b, f32x4 c) {
  return __builtin_amdgcn_mfma_f32_16x16x32_bf16(__builtin_bit_cast(bf16x8, a), __builtin_bit_cast(bf16x8, b), c, 0, 0,
                                                 0);
}
__device__ __forceinline__ void dma16(const void* g, void* l) {
  __builtin_amdgcn_global_load_lds((const unsigned*)g, (unsigned*)l, 16, 0, 0);
}

constexpr int PB_QKV = 8 * 3 * 20, PB_WO = 8 * 20, PB_GU = 27 * 2 * 20, PB_WD = 8 * 54, PB_X = 128;
constexpr int PB_TOT = PB_QKV + PB_WO + PB_GU + PB_WD + PB_X;

__global__ __launch_bounds__(256) void k_prep(const unsigned short* __restrict__ x0,
                                              const unsigned short* __restrict__ wqkv,
                                              const unsigned short* __restrict__ wphi,
                                              const unsigned short* __restrict__ wo,
                                              const unsigned short* __restrict__ wg,
                                              const unsigned short* __restrict__ wu,
                                              const unsigned short* __restrict__ wd, unsigned char* __restrict__ ws) {
  __shared__ __attribute__((aligned(16))) unsigned short tile[32 * 88];
  __shared__ __attribute__((aligned(16))) float ph[4 * 400];
  const int tid = threadIdx.x;
  int blk = blockIdx.x;
  if (blk >= PB_QKV + PB_WO + PB_GU + PB_WD) {
    const int rgG = blk - (PB_QKV + PB_WO + PB_GU + PB_WD);
    const int tok = tid & 15, rest = tid >> 4;
    const int row = rgG * 16 + tok;
    u32x4 a[3][2];
#pragma unroll
    for (int it = 0; it < 3; ++it) {
      int cgG = rest + 16 * it;
      cgG = cgG < 40 ? cgG : 0;
      const u32x4* src = (const u32x4*)(x0 + (size_t)row * D_ + cgG * 16);
      a[it][0] = src[0];
      a[it][1] = src[1];
    }
#pragma unroll
    for (int it = 0; it < 3; ++it) {
      int cgG = rest + 16 * it;
      if (cgG < 40) {
        unsigned char* dst =
            ws + XF0 + ((((size_t)(rgG >> 2) * 20 + (cgG >> 1)) * 4 + (rgG & 3)) * 64 + tok) * 16 + (cgG & 1) * 8;
        float s1 = 0.f, s2 = 0.f;
#pragma unroll
        for (int q = 0; q < 4; ++q) {
          unsigned u0 = a[it][q >> 1][(q & 1) * 2], u1 = a[it][q >> 1][(q & 1) * 2 + 1];
          *(u32x2*)(dst + q * 256) = u32x2{u0, u1};
          float e0 = lo2f(u0), e1 = hi2f(u0), e2 = lo2f(u1), e3 = hi2f(u1);
          s1 += (e0 + e1) + (e2 + e3);
          s2 += (e0 * e0 + e1 * e1) + (e2 * e2 + e3 * e3);
        }
        *(f32x2*)(ws + ST0 + ((size_t)row * 40 + cgG) * 8) = f32x2{s1, s2};
      }
    }
    return;
  }
  const int row = tid >> 3, seg = tid & 7;
  const int rq = row >> 3, rh = (row >> 2) & 1, rr = row & 3;
  const unsigned short* src;
  int ncg, fold = 0, head0 = 0;
  u32x4* dst;
  float* cs = nullptr;
  int csstride = 0;
  if (blk < PB_QKV) {
    int ks = blk % 20, t = (blk / 20) % 3, g = blk / 60;
    int feat = (2 * ks + rh) * 16 + 4 * rq + rr;
    src = wqkv + (size_t)feat * 1920 + t * 640 + g * 80;
    ncg = 5;
    fold = t < 2;
    head0 = g * 4;
    dst = (u32x4*)(ws + WQKV_T) + (size_t)blk * 320;
    cs = (float*)(ws + CSQ) + (g * 20 + ks) * 240 + t * 80;
    csstride = 16;
  } else if (blk < PB_QKV + PB_WO) {
    blk -= PB_QKV;
    int ks = blk % 20, n = blk / 20;
    int head, d;
    if (ks < 16) {
      head = 2 * ks + rh;
      d = 4 * rq + rr;
    } else {
      head = 8 * (ks - 16) + 2 * rq + rh;
      d = 16 + rr;
    }
    src = wo + (size_t)(head * 20 + d) * 640 + n * 80;
    ncg = 5;
    dst = (u32x4*)(ws + WO_T) + (size_t)blk * 320;
  } else if (blk < PB_QKV + PB_WO + PB_GU) {
    blk -= PB_QKV + PB_WO;
    int ks = blk % 20, gu = (blk / 20) & 1, cb = blk / 40;
    int feat = (2 * ks + rh) * 16 + 4 * rq + rr;
    src = (gu ? wu : wg) + (size_t)feat * DF_ + cb * 64;
    ncg = 4;
    dst = (u32x4*)(ws + WGU_T) + (size_t)((cb * 4 * 20 + ks) * 2 + gu) * 64;
    cs = (float*)(ws + CSG) + ((cb * 4 * 2 + gu) * 20 + ks) * 16;
    csstride = 2 * 20 * 16;
  } else {
    blk -= PB_QKV + PB_WO + PB_GU;
    int ks = blk % 54, n = blk / 54;
    int feat = (2 * ks + rh) * 16 + 4 * rq + rr;
    src = wd + (size_t)feat * 640 + n * 80;
    ncg = 5;
    dst = (u32x4*)(ws + WD_T) + (size_t)blk * 320;
  }
  u32x4 v0 = *(const u32x4*)(src + seg * 8);
  u32x4 v1 = *(const u32x4*)(src + ((seg < 2 && ncg == 5) ? 64 + seg * 8 : seg * 8));
  u32x4 pv = {0u, 0u, 0u, 0u};
  if (fold) pv = *(const u32x4*)(wphi + head0 * 400 + (tid < 200 ? tid : 0) * 8);
  *(u32x4*)(tile + row * 88 + seg * 8) = v0;
  if (seg < 2) *(u32x4*)(tile + row * 88 + 64 + seg * 8) = v1;
  if (fold && tid < 200) {
    *(f32x4*)(ph + tid * 8) = f32x4{lo2f(pv[0]), hi2f(pv[0]), lo2f(pv[1]), hi2f(pv[1])};
    *(f32x4*)(ph + tid * 8 + 4) = f32x4{lo2f(pv[2]), hi2f(pv[2]), lo2f(pv[3]), hi2f(pv[3])};
  }
  __syncthreads();
  const int cgstride = (ncg == 4) ? 40 * 64 : 64;
#pragma unroll
  for (int it = 0; it < 2; ++it) {
    int p = it * 256 + tid;
    if (p < ncg * 64) {
      int cg = p >> 6, lane = p & 63, i = lane & 15, q = lane >> 4;
      float v[8];
      if (fold) {
        int hh = i >> 2, f = cg * 4 + (i & 3);
        float pw[20];
#pragma unroll
        for (int d = 0; d < 20; ++d) pw[d] = ph[hh * 400 + d * 20 + f];
#pragma unroll
        for (int j = 0; j < 8; ++j) {
          const unsigned short* tr = tile + (8 * q + j) * 88 + hh * 20;
          float a = 0.f;
#pragma unroll
          for (int d = 0; d < 20; ++d) a = fmaf(us2f(tr[d]), pw[d], a);
          v[j] = a;
        }
      } else {
        int col = (blockIdx.x < PB_QKV) ? ((i >> 2) * 20 + cg * 4 + (i & 3)) : (cg * 16 + i);
#pragma unroll
        for (int j = 0; j < 8; ++j) v[j] = us2f(tile[(8 * q + j) * 88 + col]);
      }
      u32x4 o;
      o[0] = pack2(v[0], v[1]);
      o[1] = pack2(v[2], v[3]);
      o[2] = pack2(v[4], v[5]);
      o[3] = pack2(v[6], v[7]);
      dst[cg * cgstride + lane] = o;
      if (cs != nullptr) {
        float sm = ((lo2f(o[0]) + hi2f(o[0])) + (lo2f(o[1]) + hi2f(o[1]))) +
                   ((lo2f(o[2]) + hi2f(o[2])) + (lo2f(o[3]) + hi2f(o[3])));
        sm += __shfl_xor(sm, 16);
        sm += __shfl_xor(sm, 32);
        if (q == 0) cs[cg * csstride + i] = sm;
      }
    }
  }
}

__device__ __forceinline__ f32x2 row_stats(const unsigned char* __restrict__ st, int row, int part) {
  const f32x4* src = (const f32x4*)(st + (size_t)row * 320) + part * 5;
  f32x4 a0 = src[0], a1 = src[1], a2 = src[2], a3 = src[3], a4 = src[4];
  f32x4 s = ((a0 + a1) + (a2 + a3)) + a4;
  float s1 = s[0] + s[2], s2 = s[1] + s[3];
  s1 += __shfl_xor(s1, 1);
  s2 += __shfl_xor(s2, 1);
  s1 += __shfl_xor(s1, 2);
  s2 += __shfl_xor(s2, 2);
  float mean = s1 * (1.f / 640.f);
  float var = fmaxf(s2 * (1.f / 640.f) - mean * mean, 0.f);
  float rstd = rsqrtf(var + 1e-5f);
  return f32x2{rstd, mean * rstd};
}

__device__ __forceinline__ void dual_softmax(const f32x4 (&acc)[5], unsigned (&out)[20]) {
  float p[20];
#pragma unroll
  for (int c = 0; c < 5; ++c) {
    unsigned u0 = pack2(acc[c][0], acc[c][1]), u1 = pack2(acc[c][2], acc[c][3]);
    p[4 * c + 0] = lo2f(u0);
    p[4 * c + 1] = hi2f(u0);
    p[4 * c + 2] = lo2f(u1);
    p[4 * c + 3] = hi2f(u1);
  }
  float mx = p[0], mn = p[0];
#pragma unroll
  for (int f = 1; f < 20; ++f) {
    mx = fmaxf(mx, p[f]);
    mn = fminf(mn, p[f]);
  }
  const float L2E = 1.4426950408889634f;
  float e[20], n[20], s1 = 0.f, s2 = 0.f;
  float mxl = -mx * L2E, mnl = mn * L2E;
#pragma unroll
  for (int f = 0; f < 20; ++f) {
    e[f] = __builtin_amdgcn_exp2f(fmaf(p[f], L2E, mxl));
    n[f] = __builtin_amdgcn_exp2f(fmaf(p[f], -L2E, mnl));
    s1 += e[f];
    s2 += n[f];
  }
  float r1 = 1.f / s1, r2 = 1.f / s2;
#pragma unroll
  for (int f = 0; f < 10; ++f) {
    out[f] = pack2(e[2 * f] * r1, e[2 * f + 1] * r1);
    out[10 + f] = pack2(n[2 * f] * r2, n[2 * f + 1] * r2);
  }
}

constexpr int K1_RED = 0;
constexpr int K1_PHI_OFF = 6 * 20480;
constexpr int K1_V_OFF = K1_PHI_OFF + 4 * 40 * 144;
constexpr int K1_ST_OFF = K1_V_OFF + 4 * 24 * 144;
constexpr int K1_CS_OFF = K1_ST_OFF + 512;
constexpr int K1_LDS = K1_CS_OFF + 1024 + 512;
constexpr int K1_PF = 3;

__global__ __launch_bounds__(768, 3) void k_qkv(unsigned char* __restrict__ ws, int xsel) {
  extern __shared__ __attribute__((aligned(16))) unsigned char lds[];
  const int b = blockIdx.x, tid = threadIdx.x, lane = tid & 63;
  const int wave = __builtin_amdgcn_readfirstlane(tid >> 6);
  const int xcd = b & 7, idx = b >> 3;
  const int mt = (xcd >> 1) * 8 + (idx >> 2), g = (xcd & 1) * 4 + (idx & 3);
  const int tok = lane & 15, q = lane >> 4;
  const int t = wave % 3, kq = wave / 3;
  const unsigned char* xf = ws + XF0 + (size_t)xsel * O_B;
  const unsigned char* st = ws + ST0 + (size_t)xsel * ST_B;

  STAMP(ws, 1, 0);
  {
    const unsigned char* src = xf + (size_t)mt * 81920 + lane * 16;
#pragma unroll
    for (int i = 0; i < 7; ++i) {
      int pc = wave + 12 * i;
      if (pc < 80) dma16(src + pc * 1024, lds + pc * 1024);
    }
  }
  const u32x4* wp = (const u32x4*)(ws + WQKV_T) + (size_t)((g * 3 + t) * 100 + kq * 25) * 64 + lane;
  u32x4 wr[K1_PF][5];
#pragma unroll
  for (int s = 0; s < K1_PF; ++s)
#pragma unroll
    for (int c = 0; c < 5; ++c) wr[s][c] = wp[(s * 5 + c) * 64];
  if (wave < 4) {
    f32x2 r = row_stats(st, mt * 64 + wave * 16 + (lane >> 2), lane & 3);
    if ((lane & 3) == 0) *(f32x2*)(lds + K1_ST_OFF + (wave * 16 + (lane >> 2)) * 8) = r;
  } else if (wave < 8) {
    int col = tid - 256;
    col = col < 240 ? col : 0;
    const float* src = (const float*)(ws + CSQ) + g * 20 * 240 + col;
    float a[20];
#pragma unroll
    for (int k = 0; k < 20; ++k) a[k] = src[k * 240];
    float sm = 0.f;
#pragma unroll
    for (int k = 0; k < 20; ++k) sm += a[k];
    if (tid - 256 < 240) *(float*)(lds + K1_CS_OFF + col * 4) = sm;
  }
  STAMP(ws, 1, 1);
  __syncthreads();
  STAMP(ws, 1, 2);

  f32x4 acc[4][5];
#pragma unroll
  for (int rg = 0; rg < 4; ++rg)
#pragma unroll
    for (int c = 0; c < 5; ++c) acc[rg][c] = f32x4{0.f, 0.f, 0.f, 0.f};
  {
    const unsigned char* abase = lds + kq * 5 * 4096 + lane * 16;
#pragma unroll
    for (int s = 0; s < 5; ++s) {
      u32x4 a[4];
#pragma unroll
      for (int rg = 0; rg < 4; ++rg) a[rg] = *(const u32x4*)(abase + s * 4096 + rg * 1024);
#pragma unroll
      for (int c = 0; c < 5; ++c)
#pragma unroll
        for (int rg = 0; rg < 4; ++rg) acc[rg][c] = mfma16(wr[s % K1_PF][c], a[rg], acc[rg][c]);
      if (s + K1_PF < 5) {
#pragma unroll
        for (int c = 0; c < 5; ++c) wr[s % K1_PF][c] = wp[((s + K1_PF) * 5 + c) * 64];
      }
      __builtin_amdgcn_sched_barrier(0);
    }
  }
  STAMP(ws, 1, 3);
  __syncthreads();
  f32x4* red = (f32x4*)(lds + K1_RED) + lane;
  if (kq >= 2) {
    f32x4* d = red + ((kq - 2) * 3 + t) * 1280;
#pragma unroll
    for (int rg = 0; rg < 4; ++rg)
#pragma unroll
      for (int c = 0; c < 5; ++c) d[(rg * 5 + c) * 64] = acc[rg][c];
  }
  __syncthreads();
  if (kq < 2) {
    f32x4* d = red + (kq * 3 + t) * 1280;
#pragma unroll
    for (int rg = 0; rg < 4; ++rg)
#pragma unroll
      for (int c = 0; c < 5; ++c) d[(rg * 5 + c) * 64] += acc[rg][c];
  }
  __syncthreads();
  STAMP(ws, 1, 4);
  {
    const int rg = kq;
    f32x2 rs = *(const f32x2*)(lds + K1_ST_OFF + (rg * 16 + tok) * 8);
    f32x4 p[5];
#pragma unroll
    for (int c = 0; c < 5; ++c) {
      f32x4 cs = *(const f32x4*)(lds + K1_CS_OFF + (t * 80 + c * 16 + 4 * q) * 4);
      f32x4 a = red[t * 1280 + (rg * 5 + c) * 64] + red[(3 + t) * 1280 + (rg * 5 + c) * 64];
      p[c] = a * rs[0] - cs * rs[1];
    }
    if (t == 0) {
      const int head = g * 4 + q;
      unsigned o[20];
      dual_softmax(p, o);
      u32x4* dst = (u32x4*)(ws + PHIQ + ((size_t)head * 128 + mt * 4 + rg) * 1280) + tok;
#pragma unroll
      for (int c = 0; c < 5; ++c) dst[16 * c] = u32x4{o[4 * c], o[4 * c + 1], o[4 * c + 2], o[4 * c + 3]};
    } else if (t == 1) {
      unsigned o[20];
      dual_softmax(p, o);
      unsigned short* dst = (unsigned short*)(lds + K1_PHI_OFF + q * (40 * 144) + (rg * 16 + tok) * 2);
#pragma unroll
      for (int f = 0; f < 20; ++f) {
        dst[(2 * f) * 72] = (unsigned short)(o[f] & 0xffffu);
        dst[(2 * f + 1) * 72] = (unsigned short)(o[f] >> 16);
      }
    } else {
      unsigned short* dst = (unsigned short*)(lds + K1_V_OFF + q * (24 * 144) + (rg * 16 + tok) * 2);
#pragma unroll
      for (int c = 0; c < 5; ++c) {
        unsigned p0 = pack2(p[c][0], p[c][1]), p1 = pack2(p[c][2], p[c][3]);
        dst[(4 * c + 0) * 72] = (unsigned short)(p0 & 0xffffu);
        dst[(4 * c + 1) * 72] = (unsigned short)(p0 >> 16);
        dst[(4 * c + 2) * 72] = (unsigned short)(p1 & 0xffffu);
        dst[(4 * c + 3) * 72] = (unsigned short)(p1 >> 16);
      }
      dst[20 * 72] = 0x3f80;
      dst[21 * 72] = 0;
      dst[22 * 72] = 0;
      dst[23 * 72] = 0;
    }
  }
  STAMP(ws, 1, 5);
  __syncthreads();
  {
    const int hh = kq, ft = t;
    const unsigned char* vb = lds + K1_V_OFF + hh * (24 * 144) + tok * 144 + q * 16;
    const unsigned char* pb = lds + K1_PHI_OFF + hh * (40 * 144) + (ft * 16 + tok) * 144 + q * 16;
    f32x4 d0 = {0.f, 0.f, 0.f, 0.f}, d1 = {0.f, 0.f, 0.f, 0.f};
#pragma unroll
    for (int ks = 0; ks < 2; ++ks) {
      u32x4 va0 = *(const u32x4*)(vb + ks * 64);
      u32x4 va1 = *(const u32x4*)(vb + 16 * 144 + ks * 64);
      u32x4 pf = *(const u32x4*)(pb + ks * 64);
      d0 = mfma16(va0, pf, d0);
      d1 = mfma16(va1, pf, d1);
    }
    float* dst = (float*)(ws + KVP) + ((size_t)mt * 32 + g * 4 + hh) * 960;
    int f = ft * 16 + tok;
    if (f < 40) {
      *(f32x4*)(dst + f * 24 + 4 * q) = d0;
      if (q < 2) *(f32x4*)(dst + f * 24 + 16 + 4 * q) = d1;
    }
  }
  STAMP(ws, 1, 6);
}

__global__ __launch_bounds__(512) void k_attn_out(unsigned char* __restrict__ ws) {
  __shared__ __attribute__((aligned(16))) unsigned short sfr[4 * 64 * 8];
  __shared__ __attribute__((aligned(16))) float shalf[240 * 4];
  const int b = blockIdx.x, tid = threadIdx.x, lane = tid & 63;
  const int wave = __builtin_amdgcn_readfirstlane(tid >> 6);
  const int xcd = b & 7, idx = b >> 3;
  const int head = (xcd & 1) * 16 + (xcd >> 1) * 4 + (idx >> 3), mt256 = idx & 7;
  const int tok = lane & 15, q = lane >> 4;

  STAMP(ws, 2, 0);
  const int e4 = tid < 240 ? tid : (tid < 480 ? tid - 240 : 0), ph = tid < 240 ? 0 : 1;
  f32x4 part[16];
  {
    const f32x4* src = (const f32x4*)(ws + KVP) + ((size_t)ph * 16 * 32 + head) * 240 + e4;
#pragma unroll
    for (int p = 0; p < 16; ++p) part[p] = src[(size_t)p * 32 * 240];
  }
  u32x4 pq0[2], pq1[2];
#pragma unroll
  for (int i = 0; i < 2; ++i) {
    int rgG = mt256 * 16 + wave * 2 + i;
    const u32x4* src = (const u32x4*)(ws + PHIQ + ((size_t)head * 128 + rgG) * 1280);
    pq0[i] = src[lane];
    pq1[i] = src[64 + tok];
  }
  __builtin_amdgcn_sched_barrier(0);
  if (tid < 256) *(u32x4*)(sfr + tid * 8) = u32x4{0u, 0u, 0u, 0u};
  f32x4 s = part[0];
#pragma unroll
  for (int p = 1; p < 16; ++p) s += part[p];
  if (tid >= 240 && tid < 480) *(f32x4*)(shalf + e4 * 4) = s;
  STAMP(ws, 2, 1);
  __syncthreads();
  if (tid < 240) {
    s += *(const f32x4*)(shalf + e4 * 4);
    int f = tid / 6, d0 = (tid % 6) * 4;
    unsigned u0 = pack2(s[0], s[1]), u1 = pack2(s[2], s[3]);
    unsigned short* dst = sfr + (((d0 >> 4) * 2 + (f >> 5)) * 64 + ((f & 31) >> 3) * 16 + (d0 & 15)) * 8 + (f & 7);
    dst[0] = (unsigned short)(u0 & 0xffffu);
    dst[8] = (unsigned short)(u0 >> 16);
    dst[16] = (unsigned short)(u1 & 0xffffu);
    dst[24] = (unsigned short)(u1 >> 16);
  }
#pragma unroll
  for (int i = 0; i < 2; ++i)
#pragma unroll
    for (int e = 0; e < 4; ++e) pq1[i][e] = q == 0 ? pq1[i][e] : 0u;
  __syncthreads();
  STAMP(ws, 2, 2);
  u32x4 ka[2][2];
#pragma unroll
  for (int dt = 0; dt < 2; ++dt)
#pragma unroll
    for (int ks = 0; ks < 2; ++ks) ka[dt][ks] = *(const u32x4*)(sfr + ((dt * 2 + ks) * 64 + lane) * 8);
#pragma unroll
  for (int i = 0; i < 2; ++i) {
    f32x4 d0 = {0.f, 0.f, 0.f, 0.f}, d1 = {0.f, 0.f, 0.f, 0.f};
    d0 = mfma16(ka[0][0], pq0[i], d0);
    d0 = mfma16(ka[0][1], pq1[i], d0);
    d1 = mfma16(ka[1][0], pq0[i], d1);
    d1 = mfma16(ka[1][1], pq1[i], d1);
    float den = __shfl(d1[0], 16 + tok);
    den = rbf(rbf(den) + 1e-6f);
    float rd = 1.f / den;
    int rgG = mt256 * 16 + wave * 2 + i;
    int mt64 = rgG >> 2, rg = rgG & 3;
    {
      u32x2 o;
      o[0] = pack2(rbf(d0[0]) * rd, rbf(d0[1]) * rd);
      o[1] = pack2(rbf(d0[2]) * rd, rbf(d0[3]) * rd);
      unsigned char* dst = ws + O_T + (((size_t)mt64 * 20 + (head >> 1)) * 4 + rg) * 1024 + lane * 16 + (head & 1) * 8;
      *(u32x2*)dst = o;
    }
    if (q == 0) {
      u32x2 o;
      o[0] = pack2(rbf(d1[0]) * rd, rbf(d1[1]) * rd);
      o[1] = pack2(rbf(d1[2]) * rd, rbf(d1[3]) * rd);
      unsigned char* dst = ws + O_T + (((size_t)mt64 * 20 + 16 + (head >> 3)) * 4 + rg) * 1024 +
                           (((head & 7) >> 1) * 16 + tok) * 16 + (head & 1) * 8;
      *(u32x2*)dst = o;
    }
  }
  STAMP(ws, 2, 3);
}

template <int NKS, int PF>
__device__ __forceinline__ void splitk_body(const u32x4* __restrict__ wp, const u32x4* __restrict__ ap,
                                            f32x4 (&acc)[5][4]) {
  u32x4 wr[PF][5], ar[PF][4];
#pragma unroll
  for (int s = 0; s < PF; ++s) {
    if (s < NKS) {
#pragma unroll
      for (int rg = 0; rg < 4; ++rg) ar[s][rg] = ap[(s * 4 + rg) * 64];
#pragma unroll
      for (int c = 0; c < 5; ++c) wr[s][c] = wp[(s * 5 + c) * 64];
    }
  }
  __builtin_amdgcn_sched_barrier(0);
#pragma unroll
  for (int ks = 0; ks < NKS; ++ks) {
#pragma unroll
    for (int c = 0; c < 5; ++c)
#pragma unroll
      for (int rg = 0; rg < 4; ++rg) acc[c][rg] = mfma16(wr[ks % PF][c], ar[ks % PF][rg], acc[c][rg]);
    if (ks + PF < NKS) {
#pragma unroll
      for (int rg = 0; rg < 4; ++rg) ar[ks % PF][rg] = ap[((ks + PF) * 4 + rg) * 64];
#pragma unroll
      for (int c = 0; c < 5; ++c) wr[ks % PF][c] = wp[((ks + PF) * 5 + c) * 64];
    }
    __builtin_amdgcn_sched_barrier(0);
  }
}

template <int KSTOT, bool FINAL>
__global__ __launch_bounds__(512, 2) void k_proj(unsigned char* __restrict__ ws, unsigned short* __restrict__ out,
                                                 int xsel_in, int xsel_out) {
  const unsigned char* wt = ws + (KSTOT == 20 ? WO_T : WD_T);
  const unsigned char* at = ws + (KSTOT == 20 ? O_T : H_T);
  extern __shared__ __attribute__((aligned(16))) unsigned char lds[];
  const int b = blockIdx.x, tid = threadIdx.x, lane = tid & 63;
  const int wave = __builtin_amdgcn_readfirstlane(tid >> 6);
  const int xcd = b & 7, idx = b >> 3;
  const int mt = (xcd >> 1) * 8 + (idx >> 2), n = (xcd & 1) * 4 + (idx & 3);
  STAMP(ws, KSTOT == 20 ? 3 : 5, 0);
  f32x4 acc[5][4];
#pragma unroll
  for (int c = 0; c < 5; ++c)
#pragma unroll
    for (int rg = 0; rg < 4; ++rg) acc[c][rg] = f32x4{0.f, 0.f, 0.f, 0.f};
  u32x2 res[3];
  const unsigned char* xin = ws + XF0 + (size_t)xsel_in * O_B;
#pragma unroll
  for (int i = 0; i < 3; ++i) {
    int tile = i * 8 + wave;
    tile = tile < 20 ? tile : 0;
    int c = tile >> 2, rg = tile & 3;
    int cgG = n * 5 + c;
    size_t off = ((((size_t)mt * 20 + (cgG >> 1)) * 4 + rg) * 64 + lane) * 16 + (cgG & 1) * 8;
    res[i] = *(const u32x2*)(xin + off);
  }

  if (KSTOT == 20) {
    const int ks0 = wave * 2 + (wave < 4 ? wave : 4);
    const u32x4* wp = (const u32x4*)wt + (size_t)((n * 20 + ks0) * 5) * 64 + lane;
    const u32x4* ap = (const u32x4*)at + (size_t)((mt * 20 + ks0) * 4) * 64 + lane;
    if (wave < 4)
      splitk_body<3, 3>(wp, ap, acc);
    else
      splitk_body<2, 2>(wp, ap, acc);
  } else {
    const int ks0 = wave * 6 + (wave < 6 ? wave : 6);
    const u32x4* wp = (const u32x4*)wt + (size_t)((n * 54 + ks0) * 5) * 64 + lane;
    const u32x4* ap = (const u32x4*)at + (size_t)((mt * 54 + ks0) * 4) * 64 + lane;
    if (wave < 6)
      splitk_body<7, 3>(wp, ap, acc);
    else
      splitk_body<6, 3>(wp, ap, acc);
  }
  STAMP(ws, KSTOT == 20 ? 3 : 5, 1);
  f32x4* reg = (f32x4*)lds + (wave & 3) * 1280 + lane;
  if (wave >= 4) {
#pragma unroll
    for (int c = 0; c < 5; ++c)
#pragma unroll
      for (int rg = 0; rg < 4; ++rg) reg[(c * 4 + rg) * 64] = acc[c][rg];
  }
  __syncthreads();
  if (wave < 4) {
#pragma unroll
    for (int c = 0; c < 5; ++c)
#pragma unroll
      for (int rg = 0; rg < 4; ++rg) reg[(c * 4 + rg) * 64] += acc[c][rg];
  }
  __syncthreads();
  STAMP(ws, KSTOT == 20 ? 3 : 5, 2);
#pragma unroll
  for (int i = 0; i < 3; ++i) {
    int tile = i * 8 + wave;
    if (tile < 20) {
      int c = tile >> 2, rg = tile & 3;
      const f32x4* src = (const f32x4*)lds + tile * 64 + lane;
      f32x4 s = (src[0] + src[1280]) + (src[2560] + src[3840]);
      u32x2 r = res[i];
      u32x2 o;
      o[0] = pack2(lo2f(r[0]) + rbf(s[0]), hi2f(r[0]) + rbf(s[1]));
      o[1] = pack2(lo2f(r[1]) + rbf(s[2]), hi2f(r[1]) + rbf(s[3]));
      int row = mt * 64 + rg * 16 + (lane & 15);
      if (FINAL) {
        *(u32x2*)(out + (size_t)row * D_ + n * 80 + c * 16 + 4 * (lane >> 4)) = o;
      } else {
        int cgG = n * 5 + c;
        size_t off = ((((size_t)mt * 20 + (cgG >> 1)) * 4 + rg) * 64 + lane) * 16 + (cgG & 1) * 8;
        *(u32x2*)(ws + XF0 + (size_t)xsel_out * O_B + off) = o;
        float e0 = lo2f(o[0]), e1 = hi2f(o[0]), e2 = lo2f(o[1]), e3 = hi2f(o[1]);
        float s1 = (e0 + e1) + (e2 + e3);
        float s2 = (e0 * e0 + e1 * e1) + (e2 * e2 + e3 * e3);
        s1 += __shfl_xor(s1, 16);
        s2 += __shfl_xor(s2, 16);
        s1 += __shfl_xor(s1, 32);
        s2 += __shfl_xor(s2, 32);
        if (lane < 16) *(f32x2*)(ws + ST0 + (size_t)xsel_out * ST_B + ((size_t)row * 40 + cgG) * 8) = f32x2{s1, s2};
      }
    }
  }
  STAMP(ws, KSTOT == 20 ? 3 : 5, 3);
}

constexpr int K4_PF = 8;
constexpr int K4_ST_OFF = 2 * 19 * 4096;
constexpr int K4_LDS = K4_ST_OFF + 1024;
__device__ __forceinline__ unsigned geglu2(float g0, float g1, float u0, float u1) {
  unsigned gp = pack2(g0, g1), up = pack2(u0, u1);
  f32x2 g = {lo2f(gp), hi2f(gp)}, u = {lo2f(up), hi2f(up)};
  const float A = -2.302208198f, B = -0.1029432397f;
  f32x2 g2 = g * g;
  f32x2 w = g * (f32x2{B, B} * g2 + f32x2{A, A});
  f32x2 d = {1.f + __builtin_amdgcn_exp2f(w[0]), 1.f + __builtin_amdgcn_exp2f(w[1])};
  f32x2 ge = {g[0] * __builtin_amdgcn_rcpf(d[0]), g[1] * __builtin_amdgcn_rcpf(d[1])};
  unsigned gep = pack2(ge[0], ge[1]);
  f32x2 h = f32x2{lo2f(gep), hi2f(gep)} * u;
  return pack2(h[0], h[1]);
}

__global__ __launch_bounds__(512, 2) void k_mlp_up(unsigned char* __restrict__ ws, int xsel) {
  extern __shared__ __attribute__((aligned(16))) unsigned char lds[];
  const int b = blockIdx.x, lane = threadIdx.x & 63;
  const int wave = __builtin_amdgcn_readfirstlane(threadIdx.x >> 6);
  const int xcd = b & 7, idx = b >> 3;
  const int mt = (xcd >> 1) * 4 + (idx >> 3), nt = idx & 7;
  const int first = (xcd & 1) * 54 + nt * 6 + (nt < 6 ? nt : 6), cnt = nt < 6 ? 7 : 6;
  const int tok = lane & 15, q = lane >> 4;
  const bool active = wave < cnt;
  const int c = first + (active ? wave : 0);
  const unsigned char* xf = ws + XF0 + (size_t)xsel * O_B + (size_t)mt * 163840;
  const unsigned char* st = ws + ST0 + (size_t)xsel * ST_B;

  STAMP(ws, 4, 0);
#pragma unroll
  for (int i = 0; i < 19; ++i) {
    int pc = wave + 8 * i;
    int h = pc >= 76 ? 1 : 0;
    dma16(xf + (pc + 4 * h) * 1024 + lane * 16, lds + pc * 1024);
  }
  const u32x4* wp = (const u32x4*)(ws + WGU_T) + (size_t)(c * 40) * 64 + lane;
  u32x4 wr[K4_PF][2];
#pragma unroll
  for (int s = 0; s < K4_PF; ++s)
#pragma unroll
    for (int i = 0; i < 2; ++i) wr[s][i] = wp[(s * 2 + i) * 64];
  {
    f32x2 r = row_stats(st, mt * 128 + wave * 16 + (lane >> 2), lane & 3);
    if ((lane & 3) == 0) *(f32x2*)(lds + K4_ST_OFF + (wave * 16 + (lane >> 2)) * 8) = r;
  }
  f32x4 csg, csu;
  {
    const f32x4* src = (const f32x4*)(ws + CSG) + (size_t)c * 160 + q;
    int k0 = tok & 3;
    f32x4 a = {0.f, 0.f, 0.f, 0.f}, u = {0.f, 0.f, 0.f, 0.f};
#pragma unroll
    for (int k = 0; k < 5; ++k) {
      a += src[(k0 * 5 + k) * 4];
      u += src[80 + (k0 * 5 + k) * 4];
    }
#pragma unroll
    for (int e = 0; e < 4; ++e) {
      a[e] += __shfl_xor(a[e], 1);
      u[e] += __shfl_xor(u[e], 1);
      a[e] += __shfl_xor(a[e], 2);
      u[e] += __shfl_xor(u[e], 2);
    }
    csg = a;
    csu = u;
  }
  STAMP(ws, 4, 1);
  __syncthreads();
  STAMP(ws, 4, 2);
  if (!active) return;

  f32x4 accg[8], accu[8];
#pragma unroll
  for (int rg = 0; rg < 8; ++rg) {
    accg[rg] = f32x4{0.f, 0.f, 0.f, 0.f};
    accu[rg] = f32x4{0.f, 0.f, 0.f, 0.f};
  }
  const unsigned char* abase = lds + lane * 16;
  u32x4 alast[8];
#pragma unroll
  for (int ks = 0; ks < 20; ++ks) {
#pragma unroll
    for (int rg = 0; rg < 8; ++rg) {
      u32x4 a;
      if (ks < 19)
        a = *(const u32x4*)(abase + (((rg >> 2) * 19 + ks) * 4 + (rg & 3)) * 1024);
      else
        a = alast[rg];
      accg[rg] = mfma16(wr[ks % K4_PF][0], a, accg[rg]);
      accu[rg] = mfma16(wr[ks % K4_PF][1], a, accu[rg]);
    }
    if (ks + K4_PF < 20) {
#pragma unroll
      for (int i = 0; i < 2; ++i) wr[ks % K4_PF][i] = wp[((ks + K4_PF) * 2 + i) * 64];
    }
    if (ks == 13) {
#pragma unroll
      for (int rg = 0; rg < 8; ++rg)
        alast[rg] = *(const u32x4*)(xf + (((rg >> 2) * 20 + 19) * 4 + (rg & 3)) * 1024 + lane * 16);
    }
    __builtin_amdgcn_sched_barrier(0);
  }
  STAMP(ws, 4, 3);
#pragma unroll
  for (int rg = 0; rg < 8; ++rg) {
    f32x2 rs = *(const f32x2*)(lds + K4_ST_OFF + (rg * 16 + tok) * 8);
    f32x4 gv = accg[rg] * rs[0] - csg * rs[1];
    f32x4 uv = accu[rg] * rs[0] - csu * rs[1];
    u32x2 o;
    o[0] = geglu2(gv[0], gv[1], uv[0], uv[1]);
    o[1] = geglu2(gv[2], gv[3], uv[2], uv[3]);
    int mt64 = mt * 2 + (rg >> 2);
    unsigned char* dst = ws + H_T + (((size_t)mt64 * 54 + (c >> 1)) * 4 + (rg & 3)) * 1024 + lane * 16 + (c & 1) * 8;
    *(u32x2*)dst = o;
  }
  STAMP(ws, 4, 4);
}

}

extern "C" void kernel_launch(void* const* d_in, const int* in_sizes, int n_in, void* d_out, int out_size, void* d_ws,
                              size_t ws_size, hipStream_t stream) {
  const unsigned short* x0 = (const unsigned short*)d_in[0];
  const unsigned short* wqkv = (const unsigned short*)d_in[1];
  const unsigned short* wphi = (const unsigned short*)d_in[2];
  const unsigned short* wo = (const unsigned short*)d_in[3];
  const unsigned short* wg = (const unsigned short*)d_in[5];
  const unsigned short* wu = (const unsigned short*)d_in[6];
  const unsigned short* wd = (const unsigned short*)d_in[7];
  unsigned char* ws = (unsigned char*)d_ws;
  unsigned short* out = (unsigned short*)d_out;

  (void)hipFuncSetAttribute((const void*)k_qkv, hipFuncAttributeMaxDynamicSharedMemorySize, K1_LDS);
  (void)hipFuncSetAttribute((const void*)k_mlp_up, hipFuncAttributeMaxDynamicSharedMemorySize, K4_LDS);
  (void)hipFuncSetAttribute((const void*)k_proj<20, false>, hipFuncAttributeMaxDynamicSharedMemorySize, 81920);
  (void)hipFuncSetAttribute((const void*)k_proj<54, false>, hipFuncAttributeMaxDynamicSharedMemorySize, 81920);
  (void)hipFuncSetAttribute((const void*)k_proj<54, true>, hipFuncAttributeMaxDynamicSharedMemorySize, 81920);

  k_prep<<<PB_TOT, 256, 0, stream>>>(x0, wqkv, wphi, wo, wg, wu, wd, ws);
  k_qkv<<<256, 768, K1_LDS, stream>>>(ws, 0);
  k_attn_out<<<256, 512, 0, stream>>>(ws);
  k_proj<20, false><<<256, 512, 81920, stream>>>(ws, out, 0, 1);
  k_mlp_up<<<256, 512, K4_LDS, stream>>>(ws, 1);
  k_proj<54, false><<<256, 512, 81920, stream>>>(ws, out, 1, 2);
  k_qkv<<<256, 768, K1_LDS, stream>>>(ws, 2);
  k_attn_out<<<256, 512, 0, stream>>>(ws);
  k_proj<20, false><<<256, 512, 81920, stream>>>(ws, out, 2, 1);
  k_mlp_up<<<256, 512, K4_LDS, stream>>>(ws, 1);
  k_proj<54, true><<<256, 512, 81920, stream>>>(ws, out, 1, 0);
}
```

```cpp
#include <hip/hip_runtime.h>
#include <hip/hip_bf16.h>
#include <stdint.h>
#include <type_traits>

typedef __attribute__((ext_vector_type(8))) short bf16x8;
typedef __attribute__((ext_vector_type(4))) float f32x4;
typedef __attribute__((ext_vector_type(4))) unsigned u32x4;
typedef __attribute__((ext_vector_type(2))) unsigned u32x2;
typedef __attribute__((ext_vector_type(2))) __bf16 bf16x2_t;

#define STAMP(ws, kid, slot) \
  do {                       \
  } while (0)

namespace {

constexpr int S_ = 2048, D_ = 640, H_ = 32, DF_ = 1728;

constexpr size_t WQKV_T = 0;
constexpr size_t WQKV_B = (size_t)8 * 3 * 20 * 5 * 1024;
constexpr size_t WO_T = WQKV_T + WQKV_B;
constexpr size_t WO_B = (size_t)8 * 20 * 5 * 1024;
constexpr size_t WGU_T = WO_T + WO_B;
constexpr size_t WGU_B = (size_t)108 * 20 * 2 * 1024;
constexpr size_t WD_T = WGU_T + WGU_B;
constexpr size_t WD_B = (size_t)8 * 54 * 5 * 1024;
constexpr size_t PHIQ = WD_T + WD_B;
constexpr size_t PHIQ_B = (size_t)32 * 128 * 1280;
constexpr size_t KVP = PHIQ + PHIQ_B;
constexpr size_t KVP_B = (size_t)32 * 32 * 960 * 4;
constexpr size_t O_T = KVP + KVP_B;
constexpr size_t O_B = (size_t)S_ * D_ * 2;
constexpr size_t H_T = O_T + O_B;
constexpr size_t H_B = (size_t)S_ * DF_ * 2;
constexpr size_t XF0 = H_T + H_B;
constexpr size_t XF1 = XF0 + O_B;
constexpr size_t XF2 = XF1 + O_B;
constexpr size_t ST_B = (size_t)S_ * 40 * 8;
constexpr size_t ST0 = XF2 + O_B;
constexpr size_t ST1 = ST0 + ST_B;
constexpr size_t ST2 = ST1 + ST_B;
constexpr size_t CSQ = ST2 + ST_B;
constexpr size_t CSQ_B = (size_t)8 * 20 * 240 * 4;
constexpr size_t CSG = CSQ + CSQ_B;
constexpr size_t CSG_B = (size_t)108 * 2 * 20 * 16 * 4;
constexpr size_t WS_END = CSG + CSG_B;

typedef __attribute__((ext_vector_type(2))) float f32x2;

__device__ __forceinline__ unsigned pack2(float a, float b) {
  bf16x2_t v;
  v[0] = (__bf16)a;
  v[1] = (__bf16)b;
  return __builtin_bit_cast(unsigned, v);
}
__device__ __forceinline__ float lo2f(unsigned u) { return __uint_as_float(u << 16); }
__device__ __forceinline__ float hi2f(unsigned u) { return __uint_as_float(u & 0xffff0000u); }
__device__ __forceinline__ float rbf(float a) {
  bf16x2_t v;
  v[0] = (__bf16)a;
  v[1] = (__bf16)0.f;
  return lo2f(__builtin_bit_cast(unsigned, v));
}
__device__ __forceinline__ float us2f(unsigned short u) { return __uint_as_float((unsigned)u << 16); }

__device__ __forceinline__ f32x4 mfma16(u32x4 a, u32x4 b, f32x4 c) {
  return __builtin_amdgcn_mfma_f32_16x16x32_bf16(__builtin_bit_cast(bf16x8, a), __builtin_bit_cast(bf16x8, b), c, 0, 0,
                                                 0);
}
__device__ __forceinline__ void dma16(const void* g, void* l) {
  unsigned keep;
  unsigned la = __builtin_amdgcn_readfirstlane((unsigned)(size_t)(__attribute__((address_space(3))) void*)l);
  asm volatile("s_mov_b32 %0, m0\n\ts_mov_b32 m0, %2\n\ts_nop 0\n\tglobal_load_lds_dwordx4 %1, off\n\ts_mov_b32 m0, %0"
               : "=&s"(keep)
               : "v"(g), "s"(la)
               : "memory");
}

constexpr int PB_QKV = 8 * 3 * 20, PB_WO = 8 * 20, PB_GU = 27 * 2 * 20, PB_WD = 8 * 54, PB_X = 128;
constexpr int PB_TOT = PB_QKV + PB_WO + PB_GU + PB_WD + PB_X;

__global__ __launch_bounds__(256) void k_prep(const unsigned short* __restrict__ x0,
                                              const unsigned short* __restrict__ wqkv,
                                              const unsigned short* __restrict__ wphi,
                                              const unsigned short* __restrict__ wo,
                                              const unsigned short* __restrict__ wg,
                                              const unsigned short* __restrict__ wu,
                                              const unsigned short* __restrict__ wd, unsigned char* __restrict__ ws) {
  __shared__ __attribute__((aligned(16))) unsigned short tile[32 * 88];
  __shared__ __attribute__((aligned(16))) float ph[4 * 400];
  const int tid = threadIdx.x;
  int blk = blockIdx.x;
  if (blk >= PB_QKV + PB_WO + PB_GU + PB_WD) {
    const int rgG = blk - (PB_QKV + PB_WO + PB_GU + PB_WD);
    const int tok = tid & 15, rest = tid >> 4;
    const int row = rgG * 16 + tok;
    u32x4 a[3][2];
#pragma unroll
    for (int it = 0; it < 3; ++it) {
      int cgG = rest + 16 * it;
      cgG = cgG < 40 ? cgG : 0;
      const u32x4* src = (const u32x4*)(x0 + (size_t)row * D_ + cgG * 16);
      a[it][0] = src[0];
      a[it][1] = src[1];
    }
#pragma unroll
    for (int it = 0; it < 3; ++it) {
      int cgG = rest + 16 * it;
      if (cgG < 40) {
        unsigned char* dst =
            ws + XF0 + ((((size_t)(rgG >> 2) * 20 + (cgG >> 1)) * 4 + (rgG & 3)) * 64 + tok) * 16 + (cgG & 1) * 8;
        float s1 = 0.f, s2 = 0.f;
#pragma unroll
        for (int q = 0; q < 4; ++q) {
          unsigned u0 = a[it][q >> 1][(q & 1) * 2], u1 = a[it][q >> 1][(q & 1) * 2 + 1];
          *(u32x2*)(dst + q * 256) = u32x2{u0, u1};
          float e0 = lo2f(u0), e1 = hi2f(u0), e2 = lo2f(u1), e3 = hi2f(u1);
          s1 += (e0 + e1) + (e2 + e3);
          s2 += (e0 * e0 + e1 * e1) + (e2 * e2 + e3 * e3);
        }
        *(f32x2*)(ws + ST0 + ((size_t)row * 40 + cgG) * 8) = f32x2{s1, s2};
      }
    }
    return;
  }
  const int row = tid >> 3, seg = tid & 7;
  const int rq = row >> 3, rh = (row >> 2) & 1, rr = row & 3;
  const unsigned short* src;
  int ncg, fold = 0, head0 = 0;
  u32x4* dst;
  float* cs = nullptr;
  int csstride = 0;
  if (blk < PB_QKV) {
    int ks = blk % 20, t = (blk / 20) % 3, g = blk / 60;
    int feat = (2 * ks + rh) * 16 + 4 * rq + rr;
    src = wqkv + (size_t)feat * 1920 + t * 640 + g * 80;
    ncg = 5;
    fold = t < 2;
    head0 = g * 4;
    dst = (u32x4*)(ws + WQKV_T) + (size_t)blk * 320;
    cs = (float*)(ws + CSQ) + (g * 20 + ks) * 240 + t * 80;
    csstride = 16;
  } else if (blk < PB_QKV + PB_WO) {
    blk -= PB_QKV;
    int ks = blk % 20, n = blk / 20;
    int head, d;
    if (ks < 16) {
      head = 2 * ks + rh;
      d = 4 * rq + rr;
    } else {
      head = 8 * (ks - 16) + 2 * rq + rh;
      d = 16 + rr;
    }
    src = wo + (size_t)(head * 20 + d) * 640 + n * 80;
    ncg = 5;
    dst = (u32x4*)(ws + WO_T) + (size_t)blk * 320;
  } else if (blk < PB_QKV + PB_WO + PB_GU) {
    blk -= PB_QKV + PB_WO;
    int ks = blk % 20, gu = (blk / 20) & 1, cb = blk / 40;
    int feat = (2 * ks + rh) * 16 + 4 * rq + rr;
    src = (gu ? wu : wg) + (size_t)feat * DF_ + cb * 64;
    ncg = 4;
    dst = (u32x4*)(ws + WGU_T) + (size_t)((ks * 108 + cb * 4) * 2 + gu) * 64;
    cs = (float*)(ws + CSG) + ((cb * 4 * 2 + gu) * 20 + ks) * 16;
    csstride = 2 * 20 * 16;
  } else {
    blk -= PB_QKV + PB_WO + PB_GU;
    int ks = blk % 54, n = blk / 54;
    int feat = (2 * ks + rh) * 16 + 4 * rq + rr;
    src = wd + (size_t)feat * 640 + n * 80;
    ncg = 5;
    dst = (u32x4*)(ws + WD_T) + (size_t)blk * 320;
  }
  u32x4 v0 = *(const u32x4*)(src + seg * 8);
  u32x4 v1 = *(const u32x4*)(src + ((seg < 2 && ncg == 5) ? 64 + seg * 8 : seg * 8));
  u32x4 pv = {0u, 0u, 0u, 0u};
  if (fold) pv = *(const u32x4*)(wphi + head0 * 400 + (tid < 200 ? tid : 0) * 8);
  *(u32x4*)(tile + row * 88 + seg * 8) = v0;
  if (seg < 2) *(u32x4*)(tile + row * 88 + 64 + seg * 8) = v1;
  if (fold && tid < 200) {
    *(f32x4*)(ph + tid * 8) = f32x4{lo2f(pv[0]), hi2f(pv[0]), lo2f(pv[1]), hi2f(pv[1])};
    *(f32x4*)(ph + tid * 8 + 4) = f32x4{lo2f(pv[2]), hi2f(pv[2]), lo2f(pv[3]), hi2f(pv[3])};
  }
  __syncthreads();
  const int cgstride = (ncg == 4) ? 2 * 64 : 64;
#pragma unroll
  for (int it = 0; it < 2; ++it) {
    int p = it * 256 + tid;
    if (p < ncg * 64) {
      int cg = p >> 6, lane = p & 63, i = lane & 15, q = lane >> 4;
      float v[8];
      if (fold) {
        int hh = i >> 2, f = cg * 4 + (i & 3);
        float pw[20];
#pragma unroll
        for (int d = 0; d < 20; ++d) pw[d] = ph[hh * 400 + d * 20 + f];
#pragma unroll
        for (int j = 0; j < 8; ++j) {
          const u32x2* tr = (const u32x2*)(tile + (8 * q + j) * 88 + hh * 20);
          float a = 0.f;
#pragma unroll
          for (int d4 = 0; d4 < 5; ++d4) {
            u32x2 w = tr[d4];
            a = fmaf(lo2f(w[0]), pw[4 * d4 + 0], a);
            a = fmaf(hi2f(w[0]), pw[4 * d4 + 1], a);
            a = fmaf(lo2f(w[1]), pw[4 * d4 + 2], a);
            a = fmaf(hi2f(w[1]), pw[4 * d4 + 3], a);
          }
          v[j] = a;
        }
      } else {
        int col = (blockIdx.x < PB_QKV) ? ((i >> 2) * 20 + cg * 4 + (i & 3)) : (cg * 16 + i);
#pragma unroll
        for (int j = 0; j < 8; ++j) v[j] = us2f(tile[(8 * q + j) * 88 + col]);
      }
      u32x4 o;
      o[0] = pack2(v[0], v[1]);
      o[1] = pack2(v[2], v[3]);
      o[2] = pack2(v[4], v[5]);
      o[3] = pack2(v[6], v[7]);
      dst[cg * cgstride + lane] = o;
      if (cs != nullptr) {
        float sm = ((lo2f(o[0]) + hi2f(o[0])) + (lo2f(o[1]) + hi2f(o[1]))) +
                   ((lo2f(o[2]) + hi2f(o[2])) + (lo2f(o[3]) + hi2f(o[3])));
        sm += __shfl_xor(sm, 16);
        sm += __shfl_xor(sm, 32);
        if (q == 0) cs[cg * csstride + i] = sm;
      }
    }
  }
}

__device__ __forceinline__ void row_stats_load(const unsigned char* __restrict__ st, int row, int part, f32x4 (&a)[5]) {
  const f32x4* src = (const f32x4*)(st + (size_t)row * 320) + part * 5;
#pragma unroll
  for (int i = 0; i < 5; ++i) a[i] = src[i];
}
__device__ __forceinline__ f32x2 row_stats_finish(const f32x4 (&a)[5]) {
  f32x4 s = ((a[0] + a[1]) + (a[2] + a[3])) + a[4];
  float s1 = s[0] + s[2], s2 = s[1] + s[3];
  s1 += __shfl_xor(s1, 1);
  s2 += __shfl_xor(s2, 1);
  s1 += __shfl_xor(s1, 2);
  s2 += __shfl_xor(s2, 2);
  float mean = s1 * (1.f / 640.f);
  float var = fmaxf(s2 * (1.f / 640.f) - mean * mean, 0.f);
  float rstd = rsqrtf(var + 1e-5f);
  return f32x2{rstd, mean * rstd};
}

__device__ __forceinline__ void dual_softmax(const f32x4 (&acc)[5], unsigned (&out)[20]) {
  float p[20];
#pragma unroll
  for (int c = 0; c < 5; ++c) {
    unsigned u0 = pack2(acc[c][0], acc[c][1]), u1 = pack2(acc[c][2], acc[c][3]);
    p[4 * c + 0] = lo2f(u0);
    p[4 * c + 1] = hi2f(u0);
    p[4 * c + 2] = lo2f(u1);
    p[4 * c + 3] = hi2f(u1);
  }
  float mx = p[0], mn = p[0];
#pragma unroll
  for (int f = 1; f < 20; ++f) {
    mx = fmaxf(mx, p[f]);
    mn = fminf(mn, p[f]);
  }
  const float L2E = 1.4426950408889634f;
  float e[20], n[20], s1 = 0.f, s2 = 0.f;
  float mxl = -mx * L2E, mnl = mn * L2E;
#pragma unroll
  for (int f = 0; f < 20; ++f) {
    e[f] = __builtin_amdgcn_exp2f(fmaf(p[f], L2E, mxl));
    n[f] = __builtin_amdgcn_exp2f(fmaf(p[f], -L2E, mnl));
    s1 += e[f];
    s2 += n[f];
  }
  float r1 = 1.f / s1, r2 = 1.f / s2;
#pragma unroll
  for (int f = 0; f < 10; ++f) {
    out[f] = pack2(e[2 * f] * r1, e[2 * f + 1] * r1);
    out[10 + f] = pack2(n[2 * f] * r2, n[2 * f + 1] * r2);
  }
}

constexpr int K1_RED = 0;
constexpr int K1_PHI_OFF = 6 * 20480;
constexpr int K1_V_OFF = K1_PHI_OFF + 4 * 40 * 144;
constexpr int K1_ST_OFF = K1_V_OFF + 4 * 24 * 144;
constexpr int K1_CS_OFF = K1_ST_OFF + 512;
constexpr int K1_LDS = K1_CS_OFF + 1024 + 512;
constexpr int K1_PF = 2;

__global__ __launch_bounds__(768, 3) void k_qkv(unsigned char* __restrict__ ws, int xsel) {
  extern __shared__ __attribute__((aligned(16))) unsigned char lds[];
  const int b = blockIdx.x, tid = threadIdx.x, lane = tid & 63;
  const int wave = __builtin_amdgcn_readfirstlane(tid >> 6);
  const int xcd = b & 7, idx = b >> 3;
  const int mt = (xcd >> 1) * 8 + (idx >> 2), g = (xcd & 1) * 4 + (idx & 3);
  const int tok = lane & 15, q = lane >> 4;
  const int t = wave % 3, kq = wave / 3;
  const unsigned char* xf = ws + XF0 + (size_t)xsel * O_B;
  const unsigned char* st = ws + ST0 + (size_t)xsel * ST_B;

  STAMP(ws, 1, 0);
  {
    const unsigned char* src = xf + (size_t)mt * 81920 + lane * 16;
#pragma unroll
    for (int i = 0; i < 7; ++i) {
      int pc = wave + 12 * i;
      pc = pc < 80 ? pc : wave;
      dma16(src + pc * 1024, lds + pc * 1024);
    }
  }
  const u32x4* wp = (const u32x4*)(ws + WQKV_T) + (size_t)((g * 3 + t) * 100 + kq * 25) * 64 + lane;
  u32x4 wr[K1_PF][5];
#pragma unroll
  for (int s = 0; s < K1_PF; ++s) {
#pragma unroll
    for (int c = 0; c < 5; ++c) wr[s][c] = wp[(s * 5 + c) * 64];
    __builtin_amdgcn_sched_barrier(0);
  }
  STAMP(ws, 1, 1);
  asm volatile("s_waitcnt vmcnt(%0)" ::"n"(5 * K1_PF) : "memory");
  __builtin_amdgcn_s_barrier();
  asm volatile("" ::: "memory");
  STAMP(ws, 1, 2);

  f32x4 acc[4][5];
#pragma unroll
  for (int rg = 0; rg < 4; ++rg)
#pragma unroll
    for (int c = 0; c < 5; ++c) acc[rg][c] = f32x4{0.f, 0.f, 0.f, 0.f};
  {
    const unsigned char* abase = lds + kq * 5 * 4096 + lane * 16;
    u32x4 a[2][4];
#pragma unroll
    for (int rg = 0; rg < 4; ++rg) a[0][rg] = *(const u32x4*)(abase + rg * 1024);
#pragma unroll
    for (int s = 0; s < 5; ++s) {
      if (s < 4) {
#pragma unroll
        for (int rg = 0; rg < 4; ++rg) a[(s + 1) & 1][rg] = *(const u32x4*)(abase + (s + 1) * 4096 + rg * 1024);
      }
      __builtin_amdgcn_sched_barrier(0);
#pragma unroll
      for (int c = 0; c < 5; ++c)
#pragma unroll
        for (int rg = 0; rg < 4; ++rg) acc[rg][c] = mfma16(wr[s % K1_PF][c], a[s & 1][rg], acc[rg][c]);
      if (s + K1_PF < 5) {
#pragma unroll
        for (int c = 0; c < 5; ++c) wr[s % K1_PF][c] = wp[((s + K1_PF) * 5 + c) * 64];
      }
      __builtin_amdgcn_sched_barrier(0);
    }
  }
  STAMP(ws, 1, 3);
  if (wave < 4) {
    f32x4 sta[5];
    row_stats_load(st, mt * 64 + wave * 16 + (lane >> 2), lane & 3, sta);
    f32x2 r = row_stats_finish(sta);
    if ((lane & 3) == 0) *(f32x2*)(lds + K1_ST_OFF + (wave * 16 + (lane >> 2)) * 8) = r;
  } else if (wave < 8) {
    int col = tid - 256;
    col = col < 240 ? col : 0;
    const float* src = (const float*)(ws + CSQ) + g * 20 * 240 + col;
    float a[20];
#pragma unroll
    for (int k = 0; k < 20; ++k) a[k] = src[k * 240];
    float sm = 0.f;
#pragma unroll
    for (int k = 0; k < 20; ++k) sm += a[k];
    if (tid - 256 < 240) *(float*)(lds + K1_CS_OFF + col * 4) = sm;
  }
  __syncthreads();
  f32x4* red = (f32x4*)(lds + K1_RED) + lane;
  if (kq >= 2) {
    f32x4* d = red + ((kq - 2) * 3 + t) * 1280;
#pragma unroll
    for (int rg = 0; rg < 4; ++rg)
#pragma unroll
      for (int c = 0; c < 5; ++c) d[(rg * 5 + c) * 64] = acc[rg][c];
  }
  __syncthreads();
  if (kq < 2) {
    f32x4* d = red + (kq * 3 + t) * 1280;
#pragma unroll
    for (int rg = 0; rg < 4; ++rg)
#pragma unroll
      for (int c = 0; c < 5; ++c) d[(rg * 5 + c) * 64] += acc[rg][c];
  }
  __syncthreads();
  STAMP(ws, 1, 4);
  {
    const int rg = kq;
    f32x2 rs = *(const f32x2*)(lds + K1_ST_OFF + (rg * 16 + tok) * 8);
    f32x4 p[5];
#pragma unroll
    for (int c = 0; c < 5; ++c) {
      f32x4 cs = *(const f32x4*)(lds + K1_CS_OFF + (t * 80 + c * 16 + 4 * q) * 4);
      f32x4 a = red[t * 1280 + (rg * 5 + c) * 64] + red[(3 + t) * 1280 + (rg * 5 + c) * 64];
#pragma unroll
      for (int e = 0; e < 4; ++e) p[c][e] = fmaf(a[e], rs[0], -cs[e] * rs[1]);
    }
    if (t == 0) {
      const int head = g * 4 + q;
      unsigned o[20];
      dual_softmax(p, o);
      u32x4* dst = (u32x4*)(ws + PHIQ + ((size_t)head * 128 + mt * 4 + rg) * 1280) + tok;
#pragma unroll
      for (int c = 0; c < 5; ++c) dst[16 * c] = u32x4{o[4 * c], o[4 * c + 1], o[4 * c + 2], o[4 * c + 3]};
    } else if (t == 1) {
      unsigned o[20];
      dual_softmax(p, o);
      unsigned short* dst = (unsigned short*)(lds + K1_PHI_OFF + q * (40 * 144) + (rg * 16 + tok) * 2);
#pragma unroll
      for (int f = 0; f < 20; ++f) {
        dst[(2 * f) * 72] = (unsigned short)(o[f] & 0xffffu);
        dst[(2 * f + 1) * 72] = (unsigned short)(o[f] >> 16);
      }
    } else {
      unsigned short* dst = (unsigned short*)(lds + K1_V_OFF + q * (24 * 144) + (rg * 16 + tok) * 2);
#pragma unroll
      for (int c = 0; c < 5; ++c) {
        unsigned p0 = pack2(p[c][0], p[c][1]), p1 = pack2(p[c][2], p[c][3]);
        dst[(4 * c + 0) * 72] = (unsigned short)(p0 & 0xffffu);
        dst[(4 * c + 1) * 72] = (unsigned short)(p0 >> 16);
        dst[(4 * c + 2) * 72] = (unsigned short)(p1 & 0xffffu);
        dst[(4 * c + 3) * 72] = (unsigned short)(p1 >> 16);
      }
      dst[20 * 72] = 0x3f80;
      dst[21 * 72] = 0;
      dst[22 * 72] = 0;
      dst[23 * 72] = 0;
    }
  }
  STAMP(ws, 1, 5);
  __syncthreads();
  {
    const int hh = kq, ft = t;
    const unsigned char* vb = lds + K1_V_OFF + hh * (24 * 144) + tok * 144 + q * 16;
    const unsigned char* pb = lds + K1_PHI_OFF + hh * (40 * 144) + (ft * 16 + tok) * 144 + q * 16;
    f32x4 d0 = {0.f, 0.f, 0.f, 0.f}, d1 = {0.f, 0.f, 0.f, 0.f};
#pragma unroll
    for (int ks = 0; ks < 2; ++ks) {
      u32x4 va0 = *(const u32x4*)(vb + ks * 64);
      u32x4 va1 = *(const u32x4*)(vb + 16 * 144 + ks * 64);
      u32x4 pf = *(const u32x4*)(pb + ks * 64);
      d0 = mfma16(va0, pf, d0);
      d1 = mfma16(va1, pf, d1);
    }
    float* dst = (float*)(ws + KVP) + ((size_t)mt * 32 + g * 4 + hh) * 960;
    int f = ft * 16 + tok;
    if (f < 40) {
      *(f32x4*)(dst + f * 24 + 4 * q) = d0;
      if (q < 2) *(f32x4*)(dst + f * 24 + 16 + 4 * q) = d1;
    }
  }
  STAMP(ws, 1, 6);
}

__global__ __launch_bounds__(512) void k_attn_out(unsigned char* __restrict__ ws) {
  __shared__ __attribute__((aligned(16))) unsigned short sfr[4 * 64 * 8];
  __shared__ __attribute__((aligned(16))) float shalf[240 * 4];
  const int b = blockIdx.x, tid = threadIdx.x, lane = tid & 63;
  const int wave = __builtin_amdgcn_readfirstlane(tid >> 6);
  const int xcd = b & 7, idx = b >> 3;
  const int head = (xcd & 1) * 16 + (xcd >> 1) * 4 + (idx >> 3), mt256 = idx & 7;
  const int tok = lane & 15, q = lane >> 4;

  STAMP(ws, 2, 0);
  const int e4 = tid < 240 ? tid : (tid < 480 ? tid - 240 : 0), ph = tid < 240 ? 0 : 1;
  f32x4 part[16];
  {
    const f32x4* src = (const f32x4*)(ws + KVP) + ((size_t)ph * 16 * 32 + head) * 240 + e4;
#pragma unroll
    for (int p = 0; p < 16; ++p) part[p] = src[(size_t)p * 32 * 240];
  }
  u32x4 pq0[2], pq1[2];
#pragma unroll
  for (int i = 0; i < 2; ++i) {
    int rgG = mt256 * 16 + wave * 2 + i;
    const u32x4* src = (const u32x4*)(ws + PHIQ + ((size_t)head * 128 + rgG) * 1280);
    pq0[i] = src[lane];
    pq1[i] = src[64 + tok];
  }
  __builtin_amdgcn_sched_barrier(0);
  if (tid < 256) *(u32x4*)(sfr + tid * 8) = u32x4{0u, 0u, 0u, 0u};
  f32x4 s = part[0];
#pragma unroll
  for (int p = 1; p < 16; ++p) s += part[p];
  if (tid >= 240 && tid < 480) *(f32x4*)(shalf + e4 * 4) = s;
  STAMP(ws, 2, 1);
  __syncthreads();
  if (tid < 240) {
    s += *(const f32x4*)(shalf + e4 * 4);
    int f = tid / 6, d0 = (tid % 6) * 4;
    unsigned u0 = pack2(s[0], s[1]), u1 = pack2(s[2], s[3]);
    unsigned short* dst = sfr + (((d0 >> 4) * 2 + (f >> 5)) * 64 + ((f & 31) >> 3) * 16 + (d0 & 15)) * 8 + (f & 7);
    dst[0] = (unsigned short)(u0 & 0xffffu);
    dst[8] = (unsigned short)(u0 >> 16);
    dst[16] = (unsigned short)(u1 & 0xffffu);
    dst[24] = (unsigned short)(u1 >> 16);
  }
#pragma unroll
  for (int i = 0; i < 2; ++i)
#pragma unroll
    for (int e = 0; e < 4; ++e) pq1[i][e] = q == 0 ? pq1[i][e] : 0u;
  __syncthreads();
  STAMP(ws, 2, 2);
  u32x4 ka[2][2];
#pragma unroll
  for (int dt = 0; dt < 2; ++dt)
#pragma unroll
    for (int ks = 0; ks < 2; ++ks) ka[dt][ks] = *(const u32x4*)(sfr + ((dt * 2 + ks) * 64 + lane) * 8);
#pragma unroll
  for (int i = 0; i < 2; ++i) {
    f32x4 d0 = {0.f, 0.f, 0.f, 0.f}, d1 = {0.f, 0.f, 0.f, 0.f};
    d0 = mfma16(ka[0][0], pq0[i], d0);
    d0 = mfma16(ka[0][1], pq1[i], d0);
    d1 = mfma16(ka[1][0], pq0[i], d1);
    d1 = mfma16(ka[1][1], pq1[i], d1);
    float den = __shfl(d1[0], 16 + tok);
    den = rbf(rbf(den) + 1e-6f);
    float rd = 1.f / den;
    int rgG = mt256 * 16 + wave * 2 + i;
    int mt64 = rgG >> 2, rg = rgG & 3;
    {
      u32x2 o;
      o[0] = pack2(rbf(d0[0]) * rd, rbf(d0[1]) * rd);
      o[1] = pack2(rbf(d0[2]) * rd, rbf(d0[3]) * rd);
      unsigned char* dst = ws + O_T + (((size_t)mt64 * 20 + (head >> 1)) * 4 + rg) * 1024 + lane * 16 + (head & 1) * 8;
      *(u32x2*)dst = o;
    }
    if (q == 0) {
      u32x2 o;
      o[0] = pack2(rbf(d1[0]) * rd, rbf(d1[1]) * rd);
      o[1] = pack2(rbf(d1[2]) * rd, rbf(d1[3]) * rd);
      unsigned char* dst = ws + O_T + (((size_t)mt64 * 20 + 16 + (head >> 3)) * 4 + rg) * 1024 +
                           (((head & 7) >> 1) * 16 + tok) * 16 + (head & 1) * 8;
      *(u32x2*)dst = o;
    }
  }
  STAMP(ws, 2, 3);
}

template <int NKS, int PF>
__device__ __forceinline__ void splitk_body(const u32x4* __restrict__ wp, const u32x4* __restrict__ ap,
                                            f32x4 (&acc)[5][4]) {
  u32x4 wr[PF][5], ar[PF][4];
#pragma unroll
  for (int s = 0; s < PF; ++s) {
    if (s < NKS) {
#pragma unroll
      for (int rg = 0; rg < 4; ++rg) ar[s][rg] = ap[(s * 4 + rg) * 64];
#pragma unroll
      for (int c = 0; c < 5; ++c) wr[s][c] = wp[(s * 5 + c) * 64];
    }
  }
  __builtin_amdgcn_sched_barrier(0);
#pragma unroll
  for (int ks = 0; ks < NKS; ++ks) {
#pragma unroll
    for (int c = 0; c < 5; ++c)
#pragma unroll
      for (int rg = 0; rg < 4; ++rg) acc[c][rg] = mfma16(wr[ks % PF][c], ar[ks % PF][rg], acc[c][rg]);
    if (ks + PF < NKS) {
#pragma unroll
      for (int rg = 0; rg < 4; ++rg) ar[ks % PF][rg] = ap[((ks + PF) * 4 + rg) * 64];
#pragma unroll
      for (int c = 0; c < 5; ++c) wr[ks % PF][c] = wp[((ks + PF) * 5 + c) * 64];
    }
    __builtin_amdgcn_sched_barrier(0);
  }
}

template <int KSTOT, bool FINAL>
__global__ __launch_bounds__(512, 2) void k_proj(unsigned char* __restrict__ ws, unsigned short* __restrict__ out,
                                                 int xsel_in, int xsel_out) {
  const unsigned char* wt = ws + (KSTOT == 20 ? WO_T : WD_T);
  const unsigned char* at = ws + (KSTOT == 20 ? O_T : H_T);
  extern __shared__ __attribute__((aligned(16))) unsigned char lds[];
  const int b = blockIdx.x, tid = threadIdx.x, lane = tid & 63;
  const int wave = __builtin_amdgcn_readfirstlane(tid >> 6);
  const int xcd = b & 7, idx = b >> 3;
  const int mt = (xcd >> 1) * 8 + (idx >> 2), n = (xcd & 1) * 4 + (idx & 3);
  STAMP(ws, KSTOT == 20 ? 3 : 5, 0);
  f32x4 acc[5][4];
#pragma unroll
  for (int c = 0; c < 5; ++c)
#pragma unroll
    for (int rg = 0; rg < 4; ++rg) acc[c][rg] = f32x4{0.f, 0.f, 0.f, 0.f};
  u32x2 res[3];
  const unsigned char* xin = ws + XF0 + (size_t)xsel_in * O_B;
#pragma unroll
  for (int i = 0; i < 3; ++i) {
    int tile = i * 8 + wave;
    tile = tile < 20 ? tile : 0;
    int c = tile >> 2, rg = tile & 3;
    int cgG = n * 5 + c;
    size_t off = ((((size_t)mt * 20 + (cgG >> 1)) * 4 + rg) * 64 + lane) * 16 + (cgG & 1) * 8;
    res[i] = *(const u32x2*)(xin + off);
  }

  if (KSTOT == 20) {
    const int ks0 = wave * 2 + (wave < 4 ? wave : 4);
    const u32x4* wp = (const u32x4*)wt + (size_t)((n * 20 + ks0) * 5) * 64 + lane;
    const u32x4* ap = (const u32x4*)at + (size_t)((mt * 20 + ks0) * 4) * 64 + lane;
    if (wave < 4)
      splitk_body<3, 3>(wp, ap, acc);
    else
      splitk_body<2, 2>(wp, ap, acc);
  } else {
    const int ks0 = wave * 6 + (wave < 6 ? wave : 6);
    const u32x4* wp = (const u32x4*)wt + (size_t)((n * 54 + ks0) * 5) * 64 + lane;
    const u32x4* ap = (const u32x4*)at + (size_t)((mt * 54 + ks0) * 4) * 64 + lane;
    if (wave < 6)
      splitk_body<7, 3>(wp, ap, acc);
    else
      splitk_body<6, 3>(wp, ap, acc);
  }
  STAMP(ws, KSTOT == 20 ? 3 : 5, 1);
  f32x4* reg = (f32x4*)lds + (wave & 3) * 1280 + lane;
  if (wave >= 4) {
#pragma unroll
    for (int c = 0; c < 5; ++c)
#pragma unroll
      for (int rg = 0; rg < 4; ++rg) reg[(c * 4 + rg) * 64] = acc[c][rg];
  }
  __syncthreads();
  if (wave < 4) {
#pragma unroll
    for (int c = 0; c < 5; ++c)
#pragma unroll
      for (int rg = 0; rg < 4; ++rg) reg[(c * 4 + rg) * 64] += acc[c][rg];
  }
  __syncthreads();
  STAMP(ws, KSTOT == 20 ? 3 : 5, 2);
#pragma unroll
  for (int i = 0; i < 3; ++i) {
    int tile = i * 8 + wave;
    if (tile < 20) {
      int c = tile >> 2, rg = tile & 3;
      const f32x4* src = (const f32x4*)lds + tile * 64 + lane;
      f32x4 s = (src[0] + src[1280]) + (src[2560] + src[3840]);
      u32x2 r = res[i];
      u32x2 o;
      o[0] = pack2(lo2f(r[0]) + rbf(s[0]), hi2f(r[0]) + rbf(s[1]));
      o[1] = pack2(lo2f(r[1]) + rbf(s[2]), hi2f(r[1]) + rbf(s[3]));
      int row = mt * 64 + rg * 16 + (lane & 15);
      if (FINAL) {
        *(u32x2*)(out + (size_t)row * D_ + n * 80 + c * 16 + 4 * (lane >> 4)) = o;
      } else {
        int cgG = n * 5 + c;
        size_t off = ((((size_t)mt * 20 + (cgG >> 1)) * 4 + rg) * 64 + lane) * 16 + (cgG & 1) * 8;
        *(u32x2*)(ws + XF0 + (size_t)xsel_out * O_B + off) = o;
        float e0 = lo2f(o[0]), e1 = hi2f(o[0]), e2 = lo2f(o[1]), e3 = hi2f(o[1]);
        float s1 = (e0 + e1) + (e2 + e3);
        float s2 = (e0 * e0 + e1 * e1) + (e2 * e2 + e3 * e3);
        s1 += __shfl_xor(s1, 16);
        s2 += __shfl_xor(s2, 16);
        s1 += __shfl_xor(s1, 32);
        s2 += __shfl_xor(s2, 32);
        if (lane < 16) *(f32x2*)(ws + ST0 + (size_t)xsel_out * ST_B + ((size_t)row * 40 + cgG) * 8) = f32x2{s1, s2};
      }
    }
  }
  STAMP(ws, KSTOT == 20 ? 3 : 5, 3);
}

constexpr int K4_PF = 4;
constexpr int K4_ST_OFF = 2 * 19 * 4096;
constexpr int K4_LDS = K4_ST_OFF + 1024;
__device__ __forceinline__ unsigned geglu2(float g0, float g1, float u0, float u1) {
  unsigned gp = pack2(g0, g1), up = pack2(u0, u1);
  f32x2 g = {lo2f(gp), hi2f(gp)}, u = {lo2f(up), hi2f(up)};
  const float A = -2.302208198f, B = -0.1029432397f;
  f32x2 g2 = g * g;
  f32x2 w = g * (f32x2{B, B} * g2 + f32x2{A, A});
  f32x2 d = {1.f + __builtin_amdgcn_exp2f(w[0]), 1.f + __builtin_amdgcn_exp2f(w[1])};
  f32x2 ge = {g[0] * __builtin_amdgcn_rcpf(d[0]), g[1] * __builtin_amdgcn_rcpf(d[1])};
  unsigned gep = pack2(ge[0], ge[1]);
  f32x2 h = f32x2{lo2f(gep), hi2f(gep)} * u;
  return pack2(h[0], h[1]);
}

__global__ __launch_bounds__(512, 2) void k_mlp_up(unsigned char* __restrict__ ws, int xsel) {
  extern __shared__ __attribute__((aligned(16))) unsigned char lds[];
  const int b = blockIdx.x, lane = threadIdx.x & 63;
  const int wave = __builtin_amdgcn_readfirstlane(threadIdx.x >> 6);
  const int xcd = b & 7, idx = b >> 3;
  const int mt = (xcd >> 1) * 4 + (idx >> 3), nt = idx & 7;
  const int first = (xcd & 1) * 54 + nt * 6 + (nt < 6 ? nt : 6), cnt = nt < 6 ? 7 : 6;
  const int tok = lane & 15, q = lane >> 4;
  const bool active = wave < cnt;
  const int c = first + (active ? wave : 0);
  const unsigned char* xf = ws + XF0 + (size_t)xsel * O_B + (size_t)mt * 163840;
  const unsigned char* st = ws + ST0 + (size_t)xsel * ST_B;

  STAMP(ws, 4, 0);
#pragma unroll
  for (int i = 0; i < 10; ++i) {
    int p = wave + 8 * i;
    int h = p >= 40 ? 1 : 0, r = p - 40 * h;
    dma16(xf + (h * 80 + r) * 1024 + lane * 16, lds + (h * 76 + r) * 1024);
  }
  const u32x4* wp = (const u32x4*)(ws + WGU_T) + (size_t)(c * 2) * 64 + lane;
  u32x4 wr[K4_PF][2];
#pragma unroll
  for (int s = 0; s < K4_PF; ++s) {
#pragma unroll
    for (int i = 0; i < 2; ++i) wr[s][i] = wp[(s * 216 + i) * 64];
    __builtin_amdgcn_sched_barrier(0);
  }
  STAMP(ws, 4, 1);
  asm volatile("s_waitcnt vmcnt(%0)" ::"n"(2 * K4_PF) : "memory");
  __builtin_amdgcn_s_barrier();
  asm volatile("" ::: "memory");
  STAMP(ws, 4, 2);

  f32x4 accg[8], accu[8];
#pragma unroll
  for (int rg = 0; rg < 8; ++rg) {
    accg[rg] = f32x4{0.f, 0.f, 0.f, 0.f};
    accu[rg] = f32x4{0.f, 0.f, 0.f, 0.f};
  }
  const unsigned char* abase = lds + lane * 16;
  u32x4 alast[8], abuf[2][8];
  f32x4 sta[5], csa[5], csb[5];
  auto dma_late = [&](int i) {
    int p = wave + 8 * i;
    int h = p >= 36 ? 1 : 0, r = p - 36 * h + 40;
    dma16(xf + (h * 80 + r) * 1024 + lane * 16, lds + (h * 76 + r) * 1024);
  };
  auto side_loads = [&]() {
    row_stats_load(st, mt * 128 + wave * 16 + (lane >> 2), lane & 3, sta);
    const f32x4* src = (const f32x4*)(ws + CSG) + (size_t)c * 160 + q + (tok & 3) * 20;
#pragma unroll
    for (int k = 0; k < 5; ++k) {
      csa[k] = src[k * 4];
      csb[k] = src[80 + k * 4];
    }
  };
  auto half_loop = [&](auto HALF) {
    constexpr int half = decltype(HALF)::value;
#pragma unroll
    for (int k2 = 0; k2 < 10; ++k2) {
      const int ks = half * 10 + k2;
      if (ks < 9) dma_late(ks);
      if (k2 == 0) {
#pragma unroll
        for (int rg = 0; rg < 8; ++rg)
          abuf[0][rg] = *(const u32x4*)(abase + (((rg >> 2) * 19 + ks) * 4 + (rg & 3)) * 1024);
      }
      if (k2 < 9) {
#pragma unroll
        for (int rg = 0; rg < 8; ++rg) {
          if (ks + 1 < 19)
            abuf[(k2 + 1) & 1][rg] = *(const u32x4*)(abase + (((rg >> 2) * 19 + ks + 1) * 4 + (rg & 3)) * 1024);
          else
            abuf[(k2 + 1) & 1][rg] = alast[rg];
        }
      }
      __builtin_amdgcn_sched_barrier(0);
#pragma unroll
      for (int rg = 0; rg < 8; ++rg) {
        accg[rg] = mfma16(wr[ks % K4_PF][0], abuf[k2 & 1][rg], accg[rg]);
        accu[rg] = mfma16(wr[ks % K4_PF][1], abuf[k2 & 1][rg], accu[rg]);
      }
      if (ks + K4_PF < 20) {
#pragma unroll
        for (int i = 0; i < 2; ++i) wr[ks % K4_PF][i] = wp[((ks + K4_PF) * 216 + i) * 64];
      }
      if (ks == 14) {
#pragma unroll
        for (int rg = 0; rg < 8; ++rg)
          alast[rg] = *(const u32x4*)(xf + (((rg >> 2) * 20 + 19) * 4 + (rg & 3)) * 1024 + lane * 16);
      }
      if (ks == 2) side_loads();
      __builtin_amdgcn_sched_barrier(0);
    }
  };
  if (active) {
    half_loop(std::integral_constant<int, 0>{});
  } else {
#pragma unroll
    for (int i = 0; i < 9; ++i) dma_late(i);
    side_loads();
  }
  {
    f32x2 r = row_stats_finish(sta);
    if ((lane & 3) == 0) *(f32x2*)(lds + K4_ST_OFF + (wave * 16 + (lane >> 2)) * 8) = r;
  }
  if (active)
    asm volatile("s_waitcnt vmcnt(2) lgkmcnt(0)" ::: "memory");
  else
    asm volatile("s_waitcnt vmcnt(0) lgkmcnt(0)" ::: "memory");
  __builtin_amdgcn_s_barrier();
  asm volatile("" ::: "memory");
  if (!active) return;
  half_loop(std::integral_constant<int, 1>{});
  STAMP(ws, 4, 3);
  f32x4 csg = ((csa[0] + csa[1]) + (csa[2] + csa[3])) + csa[4];
  f32x4 csu = ((csb[0] + csb[1]) + (csb[2] + csb[3])) + csb[4];
#pragma unroll
  for (int e = 0; e < 4; ++e) {
    csg[e] += __shfl_xor(csg[e], 1);
    csu[e] += __shfl_xor(csu[e], 1);
    csg[e] += __shfl_xor(csg[e], 2);
    csu[e] += __shfl_xor(csu[e], 2);
  }
#pragma unroll
  for (int rg = 0; rg < 8; ++rg) {
    f32x2 rs = *(const f32x2*)(lds + K4_ST_OFF + (rg * 16 + tok) * 8);
    f32x4 gv, uv;
#pragma unroll
    for (int e = 0; e < 4; ++e) {
      gv[e] = fmaf(accg[rg][e], rs[0], -csg[e] * rs[1]);
      uv[e] = fmaf(accu[rg][e], rs[0], -csu[e] * rs[1]);
    }
    u32x2 o;
    o[0] = geglu2(gv[0], gv[1], uv[0], uv[1]);
    o[1] = geglu2(gv[2], gv[3], uv[2], uv[3]);
    int mt64 = mt * 2 + (rg >> 2);
    unsigned char* dst = ws + H_T + (((size_t)mt64 * 54 + (c >> 1)) * 4 + (rg & 3)) * 1024 + lane * 16 + (c & 1) * 8;
    *(u32x2*)dst = o;
  }
  STAMP(ws, 4, 4);
}

}

extern "C" void kernel_launch(void* const* d_in, const int* in_sizes, int n_in, void* d_out, int out_size, void* d_ws,
                              size_t ws_size, hipStream_t stream) {
  const unsigned short* x0 = (const unsigned short*)d_in[0];
  const unsigned short* wqkv = (const unsigned short*)d_in[1];
  const unsigned short* wphi = (const unsigned short*)d_in[2];
  const unsigned short* wo = (const unsigned short*)d_in[3];
  const unsigned short* wg = (const unsigned short*)d_in[5];
  const unsigned short* wu = (const unsigned short*)d_in[6];
  const unsigned short* wd = (const unsigned short*)d_in[7];
  unsigned char* ws = (unsigned char*)d_ws;
  unsigned short* out = (unsigned short*)d_out;

  (void)hipFuncSetAttribute((const void*)k_qkv, hipFuncAttributeMaxDynamicSharedMemorySize, K1_LDS);
  (void)hipFuncSetAttribute((const void*)k_mlp_up, hipFuncAttributeMaxDynamicSharedMemorySize, K4_LDS);
  (void)hipFuncSetAttribute((const void*)k_proj<20, false>, hipFuncAttributeMaxDynamicSharedMemorySize, 81920);
  (void)hipFuncSetAttribute((const void*)k_proj<54, false>, hipFuncAttributeMaxDynamicSharedMemorySize, 81920);
  (void)hipFuncSetAttribute((const void*)k_proj<54, true>, hipFuncAttributeMaxDynamicSharedMemorySize, 81920);

  k_prep<<<PB_TOT, 256, 0, stream>>>(x0, wqkv, wphi, wo, wg, wu, wd, ws);
  k_qkv<<<256, 768, K1_LDS, stream>>>(ws, 0);
  k_attn_out<<<256, 512, 0, stream>>>(ws);
  k_proj<20, false><<<256, 512, 81920, stream>>>(ws, out, 0, 1);
  k_mlp_up<<<256, 512, K4_LDS, stream>>>(ws, 1);
  k_proj<54, false><<<256, 512, 81920, stream>>>(ws, out, 1, 2);
  k_qkv<<<256, 768, K1_LDS, stream>>>(ws, 2);
  k_attn_out<<<256, 512, 0, stream>>>(ws);
  k_proj<20, false><<<256, 512, 81920, stream>>>(ws, out, 2, 1);
  k_mlp_up<<<256, 512, K4_LDS, stream>>>(ws, 1);
  k_proj<54, true><<<256, 512, 81920, stream>>>(ws, out, 1, 0);
}
```

```cpp
#include <hip/hip_runtime.h>
#include <hip/hip_bf16.h>
#include <stdint.h>
#include <type_traits>

typedef __attribute__((ext_vector_type(8))) short bf16x8;
typedef __attribute__((ext_vector_type(4))) float f32x4;
typedef __attribute__((ext_vector_type(4))) unsigned u32x4;
typedef __attribute__((ext_vector_type(2))) unsigned u32x2;
typedef __attribute__((ext_vector_type(2))) __bf16 bf16x2_t;


namespace {

constexpr int S_ = 2048, D_ = 640, H_ = 32, DF_ = 1728;

constexpr size_t WQKV_T = 0;
constexpr size_t WQKV_B = (size_t)8 * 3 * 20 * 5 * 1024;
constexpr size_t WO_T = WQKV_T + WQKV_B;
constexpr size_t WO_B = (size_t)8 * 20 * 5 * 1024;
constexpr size_t WGU_T = WO_T + WO_B;
constexpr size_t WGU_B = (size_t)108 * 20 * 2 * 1024;
constexpr size_t WD_T = WGU_T + WGU_B;
constexpr size_t WD_B = (size_t)8 * 54 * 5 * 1024;
constexpr size_t PHIQ = WD_T + WD_B;
constexpr size_t PHIQ_B = (size_t)32 * 128 * 1280;
constexpr size_t KVP = PHIQ + PHIQ_B;
constexpr size_t KVP_B = (size_t)32 * 32 * 960 * 4;
constexpr size_t O_T = KVP + KVP_B;
constexpr size_t O_B = (size_t)S_ * D_ * 2;
constexpr size_t H_T = O_T + O_B;
constexpr size_t H_B = (size_t)S_ * DF_ * 2;
constexpr size_t XF0 = H_T + H_B;
constexpr size_t XF1 = XF0 + O_B;
constexpr size_t XF2 = XF1 + O_B;
constexpr size_t ST_B = (size_t)S_ * 40 * 8;
constexpr size_t ST0 = XF2 + O_B;
constexpr size_t ST1 = ST0 + ST_B;
constexpr size_t ST2 = ST1 + ST_B;
constexpr size_t CSQ = ST2 + ST_B;
constexpr size_t CSQ_B = (size_t)8 * 20 * 240 * 4;
constexpr size_t CSG = CSQ + CSQ_B;
constexpr size_t CSG_B = (size_t)108 * 2 * 20 * 16 * 4;
constexpr size_t WS_END = CSG + CSG_B;

typedef __attribute__((ext_vector_type(2))) float f32x2;

__device__ __forceinline__ unsigned pack2(float a, float b) {
  bf16x2_t v;
  v[0] = (__bf16)a;
  v[1] = (__bf16)b;
  return __builtin_bit_cast(unsigned, v);
}
__device__ __forceinline__ float lo2f(unsigned u) { return __uint_as_float(u << 16); }
__device__ __forceinline__ float hi2f(unsigned u) { return __uint_as_float(u & 0xffff0000u); }
__device__ __forceinline__ float rbf(float a) {
  bf16x2_t v;
  v[0] = (__bf16)a;
  v[1] = (__bf16)0.f;
  return lo2f(__builtin_bit_cast(unsigned, v));
}
__device__ __forceinline__ float us2f(unsigned short u) { return __uint_as_float((unsigned)u << 16); }

__device__ __forceinline__ f32x4 mfma16(u32x4 a, u32x4 b, f32x4 c) {
  return __builtin_amdgcn_mfma_f32_16x16x32_bf16(__builtin_bit_cast(bf16x8, a), __builtin_bit_cast(bf16x8, b), c, 0, 0,
                                                 0);
}
__device__ __forceinline__ void dma16(const void* g, void* l) {
  unsigned keep;
  unsigned la = __builtin_amdgcn_readfirstlane((unsigned)(size_t)(__attribute__((address_space(3))) void*)l);
  asm volatile("s_mov_b32 %0, m0\n\ts_mov_b32 m0, %2\n\ts_nop 0\n\tglobal_load_lds_dwordx4 %1, off\n\ts_mov_b32 m0, %0"
               : "=&s"(keep)
               : "v"(g), "s"(la)
               : "memory");
}

constexpr int PB_QKV = 8 * 3 * 20, PB_WO = 8 * 20, PB_GU = 27 * 2 * 20, PB_WD = 8 * 54, PB_X = 128;
constexpr int PB_TOT = PB_QKV + PB_WO + PB_GU + PB_WD + PB_X;

__global__ __launch_bounds__(256) void k_prep(const unsigned short* __restrict__ x0,
                                              const unsigned short* __restrict__ wqkv,
                                              const unsigned short* __restrict__ wphi,
                                              const unsigned short* __restrict__ wo,
                                              const unsigned short* __restrict__ wg,
                                              const unsigned short* __restrict__ wu,
                                              const unsigned short* __restrict__ wd, unsigned char* __restrict__ ws) {
  __shared__ __attribute__((aligned(16))) unsigned short tile[32 * 88];
  __shared__ __attribute__((aligned(16))) float ph[4 * 400];
  const int tid = threadIdx.x;
  int blk = blockIdx.x;
  if (blk >= PB_QKV + PB_WO + PB_GU + PB_WD) {
    const int rgG = blk - (PB_QKV + PB_WO + PB_GU + PB_WD);
    const int tok = tid & 15, rest = tid >> 4;
    const int row = rgG * 16 + tok;
    u32x4 a[3][2];
#pragma unroll
    for (int it = 0; it < 3; ++it) {
      int cgG = rest + 16 * it;
      cgG = cgG < 40 ? cgG : 0;
      const u32x4* src = (const u32x4*)(x0 + (size_t)row * D_ + cgG * 16);
      a[it][0] = src[0];
      a[it][1] = src[1];
    }
#pragma unroll
    for (int it = 0; it < 3; ++it) {
      int cgG = rest + 16 * it;
      if (cgG < 40) {
        unsigned char* dst =
            ws + XF0 + ((((size_t)(rgG >> 2) * 20 + (cgG >> 1)) * 4 + (rgG & 3)) * 64 + tok) * 16 + (cgG & 1) * 8;
        float s1 = 0.f, s2 = 0.f;
#pragma unroll
        for (int q = 0; q < 4; ++q) {
          unsigned u0 = a[it][q >> 1][(q & 1) * 2], u1 = a[it][q >> 1][(q & 1) * 2 + 1];
          *(u32x2*)(dst + q * 256) = u32x2{u0, u1};
          float e0 = lo2f(u0), e1 = hi2f(u0), e2 = lo2f(u1), e3 = hi2f(u1);
          s1 += (e0 + e1) + (e2 + e3);
          s2 += (e0 * e0 + e1 * e1) + (e2 * e2 + e3 * e3);
        }
        *(f32x2*)(ws + ST0 + ((size_t)row * 40 + cgG) * 8) = f32x2{s1, s2};
      }
    }
    return;
  }
  const int row = tid >> 3, seg = tid & 7;
  const int rq = row >> 3, rh = (row >> 2) & 1, rr = row & 3;
  const unsigned short* src;
  int ncg, fold = 0, head0 = 0;
  u32x4* dst;
  float* cs = nullptr;
  int csstride = 0;
  if (blk < PB_QKV) {
    int ks = blk % 20, t = (blk / 20) % 3, g = blk / 60;
    int feat = (2 * ks + rh) * 16 + 4 * rq + rr;
    src = wqkv + (size_t)feat * 1920 + t * 640 + g * 80;
    ncg = 5;
    fold = t < 2;
    head0 = g * 4;
    dst = (u32x4*)(ws + WQKV_T) + (size_t)blk * 320;
    cs = (float*)(ws + CSQ) + (g * 20 + ks) * 240 + t * 80;
    csstride = 16;
  } else if (blk < PB_QKV + PB_WO) {
    blk -= PB_QKV;
    int ks = blk % 20, n = blk / 20;
    int head, d;
    if (ks < 16) {
      head = 2 * ks + rh;
      d = 4 * rq + rr;
    } else {
      head = 8 * (ks - 16) + 2 * rq + rh;
      d = 16 + rr;
    }
    src = wo + (size_t)(head * 20 + d) * 640 + n * 80;
    ncg = 5;
    dst = (u32x4*)(ws + WO_T) + (size_t)blk * 320;
  } else if (blk < PB_QKV + PB_WO + PB_GU) {
    blk -= PB_QKV + PB_WO;
    int ks = blk % 20, gu = (blk / 20) & 1, cb = blk / 40;
    int feat = (2 * ks + rh) * 16 + 4 * rq + rr;
    src = (gu ? wu : wg) + (size_t)feat * DF_ + cb * 64;
    ncg = 4;
    dst = (u32x4*)(ws + WGU_T) + (size_t)((ks * 108 + cb * 4) * 2 + gu) * 64;
    cs = (float*)(ws + CSG) + ((cb * 4 * 2 + gu) * 20 + ks) * 16;
    csstride = 2 * 20 * 16;
  } else {
    blk -= PB_QKV + PB_WO + PB_GU;
    int ks = blk % 54, n = blk / 54;
    int feat = (2 * ks + rh) * 16 + 4 * rq + rr;
    src = wd + (size_t)feat * 640 + n * 80;
    ncg = 5;
    dst = (u32x4*)(ws + WD_T) + (size_t)blk * 320;
  }
  u32x4 v0 = *(const u32x4*)(src + seg * 8);
  u32x4 v1 = *(const u32x4*)(src + ((seg < 2 && ncg == 5) ? 64 + seg * 8 : seg * 8));
  u32x4 pv = {0u, 0u, 0u, 0u};
  if (fold) pv = *(const u32x4*)(wphi + head0 * 400 + (tid < 200 ? tid : 0) * 8);
  *(u32x4*)(tile + row * 88 + seg * 8) = v0;
  if (seg < 2) *(u32x4*)(tile + row * 88 + 64 + seg * 8) = v1;
  if (fold && tid < 200) {
    *(f32x4*)(ph + tid * 8) = f32x4{lo2f(pv[0]), hi2f(pv[0]), lo2f(pv[1]), hi2f(pv[1])};
    *(f32x4*)(ph + tid * 8 + 4) = f32x4{lo2f(pv[2]), hi2f(pv[2]), lo2f(pv[3]), hi2f(pv[3])};
  }
  __syncthreads();
  const int cgstride = (ncg == 4) ? 2 * 64 : 64;
#pragma unroll
  for (int it = 0; it < 2; ++it) {
    int p = it * 256 + tid;
    if (p < ncg * 64) {
      int cg = p >> 6, lane = p & 63, i = lane & 15, q = lane >> 4;
      float v[8];
      if (fold) {
        int hh = i >> 2, f = cg * 4 + (i & 3);
        float pw[20];
#pragma unroll
        for (int d = 0; d < 20; ++d) pw[d] = ph[hh * 400 + d * 20 + f];
#pragma unroll
        for (int j = 0; j < 8; ++j) {
          const u32x2* tr = (const u32x2*)(tile + (8 * q + j) * 88 + hh * 20);
          float a = 0.f;
#pragma unroll
          for (int d4 = 0; d4 < 5; ++d4) {
            u32x2 w = tr[d4];
            a = fmaf(lo2f(w[0]), pw[4 * d4 + 0], a);
            a = fmaf(hi2f(w[0]), pw[4 * d4 + 1], a);
            a = fmaf(lo2f(w[1]), pw[4 * d4 + 2], a);
            a = fmaf(hi2f(w[1]), pw[4 * d4 + 3], a);
          }
          v[j] = a;
        }
      } else {
        int col = (blockIdx.x < PB_QKV) ? ((i >> 2) * 20 + cg * 4 + (i & 3)) : (cg * 16 + i);
#pragma unroll
        for (int j = 0; j < 8; ++j) v[j] = us2f(tile[(8 * q + j) * 88 + col]);
      }
      u32x4 o;
      o[0] = pack2(v[0], v[1]);
      o[1] = pack2(v[2], v[3]);
      o[2] = pack2(v[4], v[5]);
      o[3] = pack2(v[6], v[7]);
      dst[cg * cgstride + lane] = o;
      if (cs != nullptr) {
        float sm = ((lo2f(o[0]) + hi2f(o[0])) + (lo2f(o[1]) + hi2f(o[1]))) +
                   ((lo2f(o[2]) + hi2f(o[2])) + (lo2f(o[3]) + hi2f(o[3])));
        sm += __shfl_xor(sm, 16);
        sm += __shfl_xor(sm, 32);
        if (q == 0) cs[cg * csstride + i] = sm;
      }
    }
  }
}

__device__ __forceinline__ void row_stats_load(const unsigned char* __restrict__ st, int row, int part, f32x4 (&a)[5]) {
  const f32x4* src = (const f32x4*)(st + (size_t)row * 320) + part * 5;
#pragma unroll
  for (int i = 0; i < 5; ++i) a[i] = src[i];
}
__device__ __forceinline__ f32x2 row_stats_finish(const f32x4 (&a)[5]) {
  f32x4 s = ((a[0] + a[1]) + (a[2] + a[3])) + a[4];
  float s1 = s[0] + s[2], s2 = s[1] + s[3];
  s1 += __shfl_xor(s1, 1);
  s2 += __shfl_xor(s2, 1);
  s1 += __shfl_xor(s1, 2);
  s2 += __shfl_xor(s2, 2);
  float mean = s1 * (1.f / 640.f);
  float var = fmaxf(s2 * (1.f / 640.f) - mean * mean, 0.f);
  float rstd = rsqrtf(var + 1e-5f);
  return f32x2{rstd, mean * rstd};
}

__device__ __forceinline__ void dual_softmax(const f32x4 (&acc)[5], unsigned (&out)[20]) {
  float p[20];
#pragma unroll
  for (int c = 0; c < 5; ++c) {
    unsigned u0 = pack2(acc[c][0], acc[c][1]), u1 = pack2(acc[c][2], acc[c][3]);
    p[4 * c + 0] = lo2f(u0);
    p[4 * c + 1] = hi2f(u0);
    p[4 * c + 2] = lo2f(u1);
    p[4 * c + 3] = hi2f(u1);
  }
  float mx = p[0], mn = p[0];
#pragma unroll
  for (int f = 1; f < 20; ++f) {
    mx = fmaxf(mx, p[f]);
    mn = fminf(mn, p[f]);
  }
  const float L2E = 1.4426950408889634f;
  float e[20], n[20], s1 = 0.f, s2 = 0.f;
  float mxl = -mx * L2E, mnl = mn * L2E;
#pragma unroll
  for (int f = 0; f < 20; ++f) {
    e[f] = __builtin_amdgcn_exp2f(fmaf(p[f], L2E, mxl));
    n[f] = __builtin_amdgcn_exp2f(fmaf(p[f], -L2E, mnl));
    s1 += e[f];
    s2 += n[f];
  }
  float r1 = 1.f / s1, r2 = 1.f / s2;
#pragma unroll
  for (int f = 0; f < 10; ++f) {
    out[f] = pack2(e[2 * f] * r1, e[2 * f + 1] * r1);
    out[10 + f] = pack2(n[2 * f] * r2, n[2 * f + 1] * r2);
  }
}

constexpr int K1_RED = 0;
constexpr int K1_PHI_OFF = 6 * 20480;
constexpr int K1_V_OFF = K1_PHI_OFF + 4 * 40 * 144;
constexpr int K1_ST_OFF = K1_V_OFF + 4 * 24 * 144;
constexpr int K1_CS_OFF = K1_ST_OFF + 512;
constexpr int K1_LDS = K1_CS_OFF + 1024 + 512;
constexpr int K1_PF = 2;

__global__ __launch_bounds__(768, 3) void k_qkv(unsigned char* __restrict__ ws, int xsel) {
  extern __shared__ __attribute__((aligned(16))) unsigned char lds[];
  const int b = blockIdx.x, tid = threadIdx.x, lane = tid & 63;
  const int wave = __builtin_amdgcn_readfirstlane(tid >> 6);
  const int xcd = b & 7, idx = b >> 3;
  const int mt = (xcd >> 1) * 8 + (idx >> 2), g = (xcd & 1) * 4 + (idx & 3);
  const int tok = lane & 15, q = lane >> 4;
  const int t = wave % 3, kq = wave / 3;
  const unsigned char* xf = ws + XF0 + (size_t)xsel * O_B;
  const unsigned char* st = ws + ST0 + (size_t)xsel * ST_B;

  {
    const unsigned char* src = xf + (size_t)mt * 81920 + lane * 16;
#pragma unroll
    for (int i = 0; i < 7; ++i) {
      int pc = wave + 12 * i;
      pc = pc < 80 ? pc : wave;
      dma16(src + pc * 1024, lds + pc * 1024);
    }
  }
  const u32x4* wp = (const u32x4*)(ws + WQKV_T) + (size_t)((g * 3 + t) * 100 + kq * 25) * 64 + lane;
  u32x4 wr[K1_PF][5];
#pragma unroll
  for (int s = 0; s < K1_PF; ++s) {
#pragma unroll
    for (int c = 0; c < 5; ++c) wr[s][c] = wp[(s * 5 + c) * 64];
    __builtin_amdgcn_sched_barrier(0);
  }
  asm volatile("s_waitcnt vmcnt(%0)" ::"n"(5 * K1_PF) : "memory");
  __builtin_amdgcn_s_barrier();
  asm volatile("" ::: "memory");

  f32x4 acc[4][5];
#pragma unroll
  for (int rg = 0; rg < 4; ++rg)
#pragma unroll
    for (int c = 0; c < 5; ++c) acc[rg][c] = f32x4{0.f, 0.f, 0.f, 0.f};
  {
    const unsigned char* abase = lds + kq * 5 * 4096 + lane * 16;
    u32x4 a[2][4];
#pragma unroll
    for (int rg = 0; rg < 4; ++rg) a[0][rg] = *(const u32x4*)(abase + rg * 1024);
#pragma unroll
    for (int s = 0; s < 5; ++s) {
      if (s < 4) {
#pragma unroll
        for (int rg = 0; rg < 4; ++rg) a[(s + 1) & 1][rg] = *(const u32x4*)(abase + (s + 1) * 4096 + rg * 1024);
      }
      __builtin_amdgcn_sched_barrier(0);
#pragma unroll
      for (int c = 0; c < 5; ++c)
#pragma unroll
        for (int rg = 0; rg < 4; ++rg) acc[rg][c] = mfma16(wr[s % K1_PF][c], a[s & 1][rg], acc[rg][c]);
      if (s + K1_PF < 5) {
#pragma unroll
        for (int c = 0; c < 5; ++c) wr[s % K1_PF][c] = wp[((s + K1_PF) * 5 + c) * 64];
      }
      __builtin_amdgcn_sched_barrier(0);
    }
  }
  __syncthreads();
  f32x4* red = (f32x4*)(lds + K1_RED) + lane;
  if (kq >= 2) {
    f32x4* d = red + ((kq - 2) * 3 + t) * 1280;
#pragma unroll
    for (int rg = 0; rg < 4; ++rg)
#pragma unroll
      for (int c = 0; c < 5; ++c) d[(rg * 5 + c) * 64] = acc[rg][c];
  }
  __syncthreads();
  if (kq < 2) {
    f32x4* d = red + (kq * 3 + t) * 1280;
#pragma unroll
    for (int rg = 0; rg < 4; ++rg)
#pragma unroll
      for (int c = 0; c < 5; ++c) d[(rg * 5 + c) * 64] += acc[rg][c];
  } else if (wave < 10) {
    const int w = wave - 6;
    f32x4 sta[5];
    row_stats_load(st, mt * 64 + w * 16 + (lane >> 2), lane & 3, sta);
    f32x2 r = row_stats_finish(sta);
    if ((lane & 3) == 0) *(f32x2*)(lds + K1_ST_OFF + (w * 16 + (lane >> 2)) * 8) = r;
  } else {
    int col = (tid - 640) * 2;
    col = col < 240 ? col : 0;
    const f32x2* src = (const f32x2*)((const float*)(ws + CSQ) + g * 20 * 240 + col);
    f32x2 a[20];
#pragma unroll
    for (int k = 0; k < 20; ++k) a[k] = src[k * 120];
    float s0 = 0.f, s1 = 0.f;
#pragma unroll
    for (int k = 0; k < 20; ++k) {
      s0 += a[k][0];
      s1 += a[k][1];
    }
    if (tid - 640 < 120) *(f32x2*)(lds + K1_CS_OFF + col * 4) = f32x2{s0, s1};
  }
  __syncthreads();
  {
    const int rg = kq;
    f32x2 rs = *(const f32x2*)(lds + K1_ST_OFF + (rg * 16 + tok) * 8);
    f32x4 p[5];
#pragma unroll
    for (int c = 0; c < 5; ++c) {
      f32x4 cs = *(const f32x4*)(lds + K1_CS_OFF + (t * 80 + c * 16 + 4 * q) * 4);
      f32x4 a = red[t * 1280 + (rg * 5 + c) * 64] + red[(3 + t) * 1280 + (rg * 5 + c) * 64];
#pragma unroll
      for (int e = 0; e < 4; ++e) p[c][e] = fmaf(a[e], rs[0], -cs[e] * rs[1]);
    }
    if (t == 0) {
      const int head = g * 4 + q;
      unsigned o[20];
      dual_softmax(p, o);
      u32x4* dst = (u32x4*)(ws + PHIQ + ((size_t)head * 128 + mt * 4 + rg) * 1280) + tok;
#pragma unroll
      for (int c = 0; c < 5; ++c) dst[16 * c] = u32x4{o[4 * c], o[4 * c + 1], o[4 * c + 2], o[4 * c + 3]};
    } else if (t == 1) {
      unsigned o[20];
      dual_softmax(p, o);
      unsigned short* dst = (unsigned short*)(lds + K1_PHI_OFF + q * (40 * 144) + (rg * 16 + tok) * 2);
#pragma unroll
      for (int f = 0; f < 20; ++f) {
        dst[(2 * f) * 72] = (unsigned short)(o[f] & 0xffffu);
        dst[(2 * f + 1) * 72] = (unsigned short)(o[f] >> 16);
      }
    } else {
      unsigned short* dst = (unsigned short*)(lds + K1_V_OFF + q * (24 * 144) + (rg * 16 + tok) * 2);
#pragma unroll
      for (int c = 0; c < 5; ++c) {
        unsigned p0 = pack2(p[c][0], p[c][1]), p1 = pack2(p[c][2], p[c][3]);
        dst[(4 * c + 0) * 72] = (unsigned short)(p0 & 0xffffu);
        dst[(4 * c + 1) * 72] = (unsigned short)(p0 >> 16);
        dst[(4 * c + 2) * 72] = (unsigned short)(p1 & 0xffffu);
        dst[(4 * c + 3) * 72] = (unsigned short)(p1 >> 16);
      }
      dst[20 * 72] = 0x3f80;
      dst[21 * 72] = 0;
      dst[22 * 72] = 0;
      dst[23 * 72] = 0;
    }
  }
  __syncthreads();
  {
    const int hh = kq, ft = t;
    const unsigned char* vb = lds + K1_V_OFF + hh * (24 * 144) + tok * 144 + q * 16;
    const unsigned char* pb = lds + K1_PHI_OFF + hh * (40 * 144) + (ft * 16 + tok) * 144 + q * 16;
    f32x4 d0 = {0.f, 0.f, 0.f, 0.f}, d1 = {0.f, 0.f, 0.f, 0.f};
#pragma unroll
    for (int ks = 0; ks < 2; ++ks) {
      u32x4 va0 = *(const u32x4*)(vb + ks * 64);
      u32x4 va1 = *(const u32x4*)(vb + 16 * 144 + ks * 64);
      u32x4 pf = *(const u32x4*)(pb + ks * 64);
      d0 = mfma16(va0, pf, d0);
      d1 = mfma16(va1, pf, d1);
    }
    float* dst = (float*)(ws + KVP) + ((size_t)mt * 32 + g * 4 + hh) * 960;
    int f = ft * 16 + tok;
    if (f < 40) {
      *(f32x4*)(dst + f * 24 + 4 * q) = d0;
      if (q < 2) *(f32x4*)(dst + f * 24 + 16 + 4 * q) = d1;
    }
  }
}

__global__ __launch_bounds__(512) void k_attn_out(unsigned char* __restrict__ ws) {
  __shared__ __attribute__((aligned(16))) unsigned short sfr[4 * 64 * 8];
  __shared__ __attribute__((aligned(16))) float shalf[240 * 4];
  const int b = blockIdx.x, tid = threadIdx.x, lane = tid & 63;
  const int wave = __builtin_amdgcn_readfirstlane(tid >> 6);
  const int xcd = b & 7, idx = b >> 3;
  const int head = (xcd & 1) * 16 + (xcd >> 1) * 4 + (idx >> 3), mt256 = idx & 7;
  const int tok = lane & 15, q = lane >> 4;

  const int e4 = tid < 240 ? tid : (tid < 480 ? tid - 240 : 0), ph = tid < 240 ? 0 : 1;
  f32x4 part[16];
  {
    const f32x4* src = (const f32x4*)(ws + KVP) + ((size_t)ph * 16 * 32 + head) * 240 + e4;
#pragma unroll
    for (int p = 0; p < 16; ++p) part[p] = src[(size_t)p * 32 * 240];
  }
  u32x4 pq0[2], pq1[2];
#pragma unroll
  for (int i = 0; i < 2; ++i) {
    int rgG = mt256 * 16 + wave * 2 + i;
    const u32x4* src = (const u32x4*)(ws + PHIQ + ((size_t)head * 128 + rgG) * 1280);
    pq0[i] = src[lane];
    pq1[i] = src[64 + tok];
  }
  __builtin_amdgcn_sched_barrier(0);
  if (tid < 256) *(u32x4*)(sfr + tid * 8) = u32x4{0u, 0u, 0u, 0u};
  f32x4 s = part[0];
#pragma unroll
  for (int p = 1; p < 16; ++p) s += part[p];
  if (tid >= 240 && tid < 480) *(f32x4*)(shalf + e4 * 4) = s;
  __syncthreads();
  if (tid < 240) {
    s += *(const f32x4*)(shalf + e4 * 4);
    int f = tid / 6, d0 = (tid % 6) * 4;
    unsigned u0 = pack2(s[0], s[1]), u1 = pack2(s[2], s[3]);
    unsigned short* dst = sfr + (((d0 >> 4) * 2 + (f >> 5)) * 64 + ((f & 31) >> 3) * 16 + (d0 & 15)) * 8 + (f & 7);
    dst[0] = (unsigned short)(u0 & 0xffffu);
    dst[8] = (unsigned short)(u0 >> 16);
    dst[16] = (unsigned short)(u1 & 0xffffu);
    dst[24] = (unsigned short)(u1 >> 16);
  }
#pragma unroll
  for (int i = 0; i < 2; ++i)
#pragma unroll
    for (int e = 0; e < 4; ++e) pq1[i][e] = q == 0 ? pq1[i][e] : 0u;
  __syncthreads();
  u32x4 ka[2][2];
#pragma unroll
  for (int dt = 0; dt < 2; ++dt)
#pragma unroll
    for (int ks = 0; ks < 2; ++ks) ka[dt][ks] = *(const u32x4*)(sfr + ((dt * 2 + ks) * 64 + lane) * 8);
#pragma unroll
  for (int i = 0; i < 2; ++i) {
    f32x4 d0 = {0.f, 0.f, 0.f, 0.f}, d1 = {0.f, 0.f, 0.f, 0.f};
    d0 = mfma16(ka[0][0], pq0[i], d0);
    d0 = mfma16(ka[0][1], pq1[i], d0);
    d1 = mfma16(ka[1][0], pq0[i], d1);
    d1 = mfma16(ka[1][1], pq1[i], d1);
    float den = __shfl(d1[0], 16 + tok);
    den = rbf(rbf(den) + 1e-6f);
    float rd = 1.f / den;
    int rgG = mt256 * 16 + wave * 2 + i;
    int mt64 = rgG >> 2, rg = rgG & 3;
    {
      u32x2 o;
      o[0] = pack2(rbf(d0[0]) * rd, rbf(d0[1]) * rd);
      o[1] = pack2(rbf(d0[2]) * rd, rbf(d0[3]) * rd);
      unsigned char* dst = ws + O_T + (((size_t)mt64 * 20 + (head >> 1)) * 4 + rg) * 1024 + lane * 16 + (head & 1) * 8;
      *(u32x2*)dst = o;
    }
    if (q == 0) {
      u32x2 o;
      o[0] = pack2(rbf(d1[0]) * rd, rbf(d1[1]) * rd);
      o[1] = pack2(rbf(d1[2]) * rd, rbf(d1[3]) * rd);
      unsigned char* dst = ws + O_T + (((size_t)mt64 * 20 + 16 + (head >> 3)) * 4 + rg) * 1024 +
                           (((head & 7) >> 1) * 16 + tok) * 16 + (head & 1) * 8;
      *(u32x2*)dst = o;
    }
  }
}

template <int NKS, int PF>
__device__ __forceinline__ void splitk_body(const u32x4* __restrict__ wp, const u32x4* __restrict__ ap,
                                            f32x4 (&acc)[5][4]) {
  u32x4 wr[PF][5], ar[PF][4];
#pragma unroll
  for (int s = 0; s < PF; ++s) {
    if (s < NKS) {
#pragma unroll
      for (int rg = 0; rg < 4; ++rg) ar[s][rg] = ap[(s * 4 + rg) * 64];
#pragma unroll
      for (int c = 0; c < 5; ++c) wr[s][c] = wp[(s * 5 + c) * 64];
    }
  }
  __builtin_amdgcn_sched_barrier(0);
#pragma unroll
  for (int ks = 0; ks < NKS; ++ks) {
#pragma unroll
    for (int c = 0; c < 5; ++c)
#pragma unroll
      for (int rg = 0; rg < 4; ++rg) acc[c][rg] = mfma16(wr[ks % PF][c], ar[ks % PF][rg], acc[c][rg]);
    if (ks + PF < NKS) {
#pragma unroll
      for (int rg = 0; rg < 4; ++rg) ar[ks % PF][rg] = ap[((ks + PF) * 4 + rg) * 64];
#pragma unroll
      for (int c = 0; c < 5; ++c) wr[ks % PF][c] = wp[((ks + PF) * 5 + c) * 64];
    }
    __builtin_amdgcn_sched_barrier(0);
  }
}

template <int KSTOT, bool FINAL>
__global__ __launch_bounds__(512, 2) void k_proj(unsigned char* __restrict__ ws, unsigned short* __restrict__ out,
                                                 int xsel_in, int xsel_out) {
  const unsigned char* wt = ws + (KSTOT == 20 ? WO_T : WD_T);
  const unsigned char* at = ws + (KSTOT == 20 ? O_T : H_T);
  extern __shared__ __attribute__((aligned(16))) unsigned char lds[];
  const int b = blockIdx.x, tid = threadIdx.x, lane = tid & 63;
  const int wave = __builtin_amdgcn_readfirstlane(tid >> 6);
  const int xcd = b & 7, idx = b >> 3;
  const int mt = (xcd >> 1) * 8 + (idx >> 2), n = (xcd & 1) * 4 + (idx & 3);
  f32x4 acc[5][4];
#pragma unroll
  for (int c = 0; c < 5; ++c)
#pragma unroll
    for (int rg = 0; rg < 4; ++rg) acc[c][rg] = f32x4{0.f, 0.f, 0.f, 0.f};
  u32x2 res[3];
  const unsigned char* xin = ws + XF0 + (size_t)xsel_in * O_B;
#pragma unroll
  for (int i = 0; i < 3; ++i) {
    int tile = i * 8 + wave;
    tile = tile < 20 ? tile : 0;
    int c = tile >> 2, rg = tile & 3;
    int cgG = n * 5 + c;
    size_t off = ((((size_t)mt * 20 + (cgG >> 1)) * 4 + rg) * 64 + lane) * 16 + (cgG & 1) * 8;
    res[i] = *(const u32x2*)(xin + off);
  }

  if (KSTOT == 20) {
    const int ks0 = wave * 2 + (wave < 4 ? wave : 4);
    const u32x4* wp = (const u32x4*)wt + (size_t)((n * 20 + ks0) * 5) * 64 + lane;
    const u32x4* ap = (const u32x4*)at + (size_t)((mt * 20 + ks0) * 4) * 64 + lane;
    if (wave < 4)
      splitk_body<3, 3>(wp, ap, acc);
    else
      splitk_body<2, 2>(wp, ap, acc);
  } else {
    const int ks0 = wave * 6 + (wave < 6 ? wave : 6);
    const u32x4* wp = (const u32x4*)wt + (size_t)((n * 54 + ks0) * 5) * 64 + lane;
    const u32x4* ap = (const u32x4*)at + (size_t)((mt * 54 + ks0) * 4) * 64 + lane;
    if (wave < 6)
      splitk_body<7, 3>(wp, ap, acc);
    else
      splitk_body<6, 3>(wp, ap, acc);
  }
  f32x4* reg = (f32x4*)lds + (wave & 3) * 1280 + lane;
  if (wave >= 4) {
#pragma unroll
    for (int c = 0; c < 5; ++c)
#pragma unroll
      for (int rg = 0; rg < 4; ++rg) reg[(c * 4 + rg) * 64] = acc[c][rg];
  }
  __syncthreads();
  if (wave < 4) {
#pragma unroll
    for (int c = 0; c < 5; ++c)
#pragma unroll
      for (int rg = 0; rg < 4; ++rg) reg[(c * 4 + rg) * 64] += acc[c][rg];
  }
  __syncthreads();
#pragma unroll
  for (int i = 0; i < 3; ++i) {
    int tile = i * 8 + wave;
    if (tile < 20) {
      int c = tile >> 2, rg = tile & 3;
      const f32x4* src = (const f32x4*)lds + tile * 64 + lane;
      f32x4 s = (src[0] + src[1280]) + (src[2560] + src[3840]);
      u32x2 r = res[i];
      u32x2 o;
      o[0] = pack2(lo2f(r[0]) + rbf(s[0]), hi2f(r[0]) + rbf(s[1]));
      o[1] = pack2(lo2f(r[1]) + rbf(s[2]), hi2f(r[1]) + rbf(s[3]));
      int row = mt * 64 + rg * 16 + (lane & 15);
      if (FINAL) {
        *(u32x2*)(out + (size_t)row * D_ + n * 80 + c * 16 + 4 * (lane >> 4)) = o;
      } else {
        int cgG = n * 5 + c;
        size_t off = ((((size_t)mt * 20 + (cgG >> 1)) * 4 + rg) * 64 + lane) * 16 + (cgG & 1) * 8;
        *(u32x2*)(ws + XF0 + (size_t)xsel_out * O_B + off) = o;
        float e0 = lo2f(o[0]), e1 = hi2f(o[0]), e2 = lo2f(o[1]), e3 = hi2f(o[1]);
        float s1 = (e0 + e1) + (e2 + e3);
        float s2 = (e0 * e0 + e1 * e1) + (e2 * e2 + e3 * e3);
        s1 += __shfl_xor(s1, 16);
        s2 += __shfl_xor(s2, 16);
        s1 += __shfl_xor(s1, 32);
        s2 += __shfl_xor(s2, 32);
        if (lane < 16) *(f32x2*)(ws + ST0 + (size_t)xsel_out * ST_B + ((size_t)row * 40 + cgG) * 8) = f32x2{s1, s2};
      }
    }
  }
}

constexpr int K4_PF = 4;
constexpr int K4_ST_OFF = 2 * 19 * 4096;
constexpr int K4_LDS = K4_ST_OFF + 1024;
__device__ __forceinline__ unsigned geglu2(float g0, float g1, float u0, float u1) {
  unsigned gp = pack2(g0, g1), up = pack2(u0, u1);
  f32x2 g = {lo2f(gp), hi2f(gp)}, u = {lo2f(up), hi2f(up)};
  const float A = -2.302208198f, B = -0.1029432397f;
  f32x2 g2 = g * g;
  f32x2 w = g * (f32x2{B, B} * g2 + f32x2{A, A});
  f32x2 d = {1.f + __builtin_amdgcn_exp2f(w[0]), 1.f + __builtin_amdgcn_exp2f(w[1])};
  f32x2 ge = {g[0] * __builtin_amdgcn_rcpf(d[0]), g[1] * __builtin_amdgcn_rcpf(d[1])};
  unsigned gep = pack2(ge[0], ge[1]);
  f32x2 h = f32x2{lo2f(gep), hi2f(gep)} * u;
  return pack2(h[0], h[1]);
}

__global__ __launch_bounds__(512, 2) void k_mlp_up(unsigned char* __restrict__ ws, int xsel) {
  extern __shared__ __attribute__((aligned(16))) unsigned char lds[];
  const int b = blockIdx.x, lane = threadIdx.x & 63;
  const int wave = __builtin_amdgcn_readfirstlane(threadIdx.x >> 6);
  const int xcd = b & 7, idx = b >> 3;
  const int mt = (xcd >> 1) * 4 + (idx >> 3), nt = idx & 7;
  const int first = (xcd & 1) * 54 + nt * 6 + (nt < 6 ? nt : 6), cnt = nt < 6 ? 7 : 6;
  const int tok = lane & 15, q = lane >> 4;
  const bool active = wave < cnt;
  const int c = first + (active ? wave : 0);
  const unsigned char* xf = ws + XF0 + (size_t)xsel * O_B + (size_t)mt * 163840;
  const unsigned char* st = ws + ST0 + (size_t)xsel * ST_B;

#pragma unroll
  for (int i = 0; i < 10; ++i) {
    int p = wave + 8 * i;
    int h = p >= 40 ? 1 : 0, r = p - 40 * h;
    dma16(xf + (h * 80 + r) * 1024 + lane * 16, lds + (h * 76 + r) * 1024);
  }
  const u32x4* wp = (const u32x4*)(ws + WGU_T) + (size_t)(c * 2) * 64 + lane;
  u32x4 wr[K4_PF][2];
#pragma unroll
  for (int s = 0; s < K4_PF; ++s) {
#pragma unroll
    for (int i = 0; i < 2; ++i) wr[s][i] = wp[(s * 216 + i) * 64];
    __builtin_amdgcn_sched_barrier(0);
  }
  asm volatile("s_waitcnt vmcnt(%0)" ::"n"(2 * K4_PF) : "memory");
  __builtin_amdgcn_s_barrier();
  asm volatile("" ::: "memory");

  f32x4 accg[8], accu[8];
#pragma unroll
  for (int rg = 0; rg < 8; ++rg) {
    accg[rg] = f32x4{0.f, 0.f, 0.f, 0.f};
    accu[rg] = f32x4{0.f, 0.f, 0.f, 0.f};
  }
  const unsigned char* abase = lds + lane * 16;
  u32x4 alast[8], abuf[2][8];
  f32x4 sta[5], csa[5], csb[5];
  auto dma_late = [&](int i) {
    int p = wave + 8 * i;
    int h = p >= 36 ? 1 : 0, r = p - 36 * h + 40;
    dma16(xf + (h * 80 + r) * 1024 + lane * 16, lds + (h * 76 + r) * 1024);
  };
  auto side_loads = [&]() {
    row_stats_load(st, mt * 128 + wave * 16 + (lane >> 2), lane & 3, sta);
    const f32x4* src = (const f32x4*)(ws + CSG) + (size_t)c * 160 + q + (tok & 3) * 20;
#pragma unroll
    for (int k = 0; k < 5; ++k) {
      csa[k] = src[k * 4];
      csb[k] = src[80 + k * 4];
    }
  };
  auto half_loop = [&](auto HALF) {
    constexpr int half = decltype(HALF)::value;
#pragma unroll
    for (int k2 = 0; k2 < 10; ++k2) {
      const int ks = half * 10 + k2;
      if (ks < 9) dma_late(ks);
      if (k2 == 0) {
#pragma unroll
        for (int rg = 0; rg < 8; ++rg)
          abuf[0][rg] = *(const u32x4*)(abase + (((rg >> 2) * 19 + ks) * 4 + (rg & 3)) * 1024);
      }
      if (k2 < 9) {
#pragma unroll
        for (int rg = 0; rg < 8; ++rg) {
          if (ks + 1 < 19)
            abuf[(k2 + 1) & 1][rg] = *(const u32x4*)(abase + (((rg >> 2) * 19 + ks + 1) * 4 + (rg & 3)) * 1024);
          else
            abuf[(k2 + 1) & 1][rg] = alast[rg];
        }
      }
      __builtin_amdgcn_sched_barrier(0);
#pragma unroll
      for (int rg = 0; rg < 8; ++rg) {
        accg[rg] = mfma16(wr[ks % K4_PF][0], abuf[k2 & 1][rg], accg[rg]);
        accu[rg] = mfma16(wr[ks % K4_PF][1], abuf[k2 & 1][rg], accu[rg]);
      }
      if (ks + K4_PF < 20) {
#pragma unroll
        for (int i = 0; i < 2; ++i) wr[ks % K4_PF][i] = wp[((ks + K4_PF) * 216 + i) * 64];
      }
      if (ks == 14) {
#pragma unroll
        for (int rg = 0; rg < 8; ++rg)
          alast[rg] = *(const u32x4*)(xf + (((rg >> 2) * 20 + 19) * 4 + (rg & 3)) * 1024 + lane * 16);
      }
      if (ks == 2) side_loads();
      __builtin_amdgcn_sched_barrier(0);
    }
  };
  if (active) {
    half_loop(std::integral_constant<int, 0>{});
  } else {
#pragma unroll
    for (int i = 0; i < 9; ++i) dma_late(i);
    side_loads();
  }
  {
    f32x2 r = row_stats_finish(sta);
    if ((lane & 3) == 0) *(f32x2*)(lds + K4_ST_OFF + (wave * 16 + (lane >> 2)) * 8) = r;
  }
  if (active)
    asm volatile("s_waitcnt vmcnt(2) lgkmcnt(0)" ::: "memory");
  else
    asm volatile("s_waitcnt vmcnt(0) lgkmcnt(0)" ::: "memory");
  __builtin_amdgcn_s_barrier();
  asm volatile("" ::: "memory");
  if (!active) return;
  half_loop(std::integral_constant<int, 1>{});
  f32x4 csg = ((csa[0] + csa[1]) + (csa[2] + csa[3])) + csa[4];
  f32x4 csu = ((csb[0] + csb[1]) + (csb[2] + csb[3])) + csb[4];
#pragma unroll
  for (int e = 0; e < 4; ++e) {
    csg[e] += __shfl_xor(csg[e], 1);
    csu[e] += __shfl_xor(csu[e], 1);
    csg[e] += __shfl_xor(csg[e], 2);
    csu[e] += __shfl_xor(csu[e], 2);
  }
#pragma unroll
  for (int rg = 0; rg < 8; ++rg) {
    f32x2 rs = *(const f32x2*)(lds + K4_ST_OFF + (rg * 16 + tok) * 8);
    f32x4 gv, uv;
#pragma unroll
    for (int e = 0; e < 4; ++e) {
      gv[e] = fmaf(accg[rg][e], rs[0], -csg[e] * rs[1]);
      uv[e] = fmaf(accu[rg][e], rs[0], -csu[e] * rs[1]);
    }
    u32x2 o;
    o[0] = geglu2(gv[0], gv[1], uv[0], uv[1]);
    o[1] = geglu2(gv[2], gv[3], uv[2], uv[3]);
    int mt64 = mt * 2 + (rg >> 2);
    unsigned char* dst = ws + H_T + (((size_t)mt64 * 54 + (c >> 1)) * 4 + (rg & 3)) * 1024 + lane * 16 + (c & 1) * 8;
    *(u32x2*)dst = o;
  }
}

}

extern "C" void kernel_launch(void* const* d_in, const int* in_sizes, int n_in, void* d_out, int out_size, void* d_ws,
                              size_t ws_size, hipStream_t stream) {
  const unsigned short* x0 = (const unsigned short*)d_in[0];
  const unsigned short* wqkv = (const unsigned short*)d_in[1];
  const unsigned short* wphi = (const unsigned short*)d_in[2];
  const unsigned short* wo = (const unsigned short*)d_in[3];
  const unsigned short* wg = (const unsigned short*)d_in[5];
  const unsigned short* wu = (const unsigned short*)d_in[6];
  const unsigned short* wd = (const unsigned short*)d_in[7];
  unsigned char* ws = (unsigned char*)d_ws;
  unsigned short* out = (unsigned short*)d_out;

  (void)hipFuncSetAttribute((const void*)k_qkv, hipFuncAttributeMaxDynamicSharedMemorySize, K1_LDS);
  (void)hipFuncSetAttribute((const void*)k_mlp_up, hipFuncAttributeMaxDynamicSharedMemorySize, K4_LDS);
  (void)hipFuncSetAttribute((const void*)k_proj<20, false>, hipFuncAttributeMaxDynamicSharedMemorySize, 81920);
  (void)hipFuncSetAttribute((const void*)k_proj<54, false>, hipFuncAttributeMaxDynamicSharedMemorySize, 81920);
  (void)hipFuncSetAttribute((const void*)k_proj<54, true>, hipFuncAttributeMaxDynamicSharedMemorySize, 81920);

  k_prep<<<PB_TOT, 256, 0, stream>>>(x0, wqkv, wphi, wo, wg, wu, wd, ws);
  k_qkv<<<256, 768, K1_LDS, stream>>>(ws, 0);
  k_attn_out<<<256, 512, 0, stream>>>(ws);
  k_proj<20, false><<<256, 512, 81920, stream>>>(ws, out, 0, 1);
  k_mlp_up<<<256, 512, K4_LDS, stream>>>(ws, 1);
  k_proj<54, false><<<256, 512, 81920, stream>>>(ws, out, 1, 2);
  k_qkv<<<256, 768, K1_LDS, stream>>>(ws, 2);
  k_attn_out<<<256, 512, 0, stream>>>(ws);
  k_proj<20, false><<<256, 512, 81920, stream>>>(ws, out, 2, 1);
  k_mlp_up<<<256, 512, K4_LDS, stream>>>(ws, 1);
  k_proj<54, true><<<256, 512, 81920, stream>>>(ws, out, 1, 0);
}
```
